```python
import torch

def _softmax(x, axis=-1):
    return torch.softmax(x, dim=axis)
def _gelu(x):
    return torch.nn.functional.gelu(x, approximate="tanh")
def _silu(x):
    return torch.nn.functional.silu(x)
def _sigmoid(x):
    return torch.sigmoid(x)
def _layernorm(x, axis=-1, eps=1e-5):
    m = x.mean(dim=axis, keepdim=True)
    v = x.var(dim=axis, keepdim=True, unbiased=False)
    return (x - m) / torch.sqrt(v + eps)
def _scatter_add(tgt, idx, src, axis=0):
    return tgt.clone().index_add_(axis, idx, src)
def _segment_sum(src, seg, n):
    out = torch.zeros((n,) + src.shape[1:], dtype=src.dtype)
    return out.index_add_(0, seg, src)
def _pad(x, pw):
    flat = tuple(v for pair in reversed(pw) for v in pair)
    return torch.nn.functional.pad(x, flat)

kernel_name = 'recurrent_scan__linear_attn_block__glu_gated_geglu__prompt'

def setup_inputs():

    _w = (lambda a: (lambda t: t.to(torch.bfloat16) if t.is_floating_point() else t)(torch.as_tensor(a)))
    def _A_setup():
        B, S, D = 1, 2048, 512
        return {
            "x": _w((lambda *s: torch.randn(*s, dtype=torch.bfloat16) * 0.05)(B, S, D)),
            "wqkv": _w((lambda *s: torch.randn(*s, dtype=torch.bfloat16) * 0.8)(D, 3 * D)),
            "wo": _w((lambda *s: torch.randn(*s, dtype=torch.bfloat16) * 0.8)(D, D)),
        }
    _A_in = _A_setup()

    def _B_setup():
        B, S, Dm, Df = 1, 2048, 512, 1376
        return {
            "x": _w((lambda *s: torch.randn(*s, dtype=torch.bfloat16) * 0.05)(B, S, Dm)),
            "w_gate": _w((lambda *s: torch.randn(*s, dtype=torch.bfloat16) * 0.2)(Dm, Df) * (Dm ** -0.5)),
            "w_up": _w((lambda *s: torch.randn(*s, dtype=torch.bfloat16) * 0.2)(Dm, Df) * (Dm ** -0.5)),
            "w_down": _w((lambda *s: torch.randn(*s, dtype=torch.bfloat16) * 0.2)(Df, Dm) * (Df ** -0.5)),
        }
    _B_in = _B_setup()

    return {
        "A_x": _A_in["x"],
        "A_wqkv": _A_in["wqkv"],
        "A_wo": _A_in["wo"],
        "B_x": _B_in["x"],
        "B_w_gate": _B_in["w_gate"],
        "B_w_up": _B_in["w_up"],
        "B_w_down": _B_in["w_down"],
    }

def reference(A_x, A_wqkv, A_wo, B_x, B_w_gate, B_w_up, B_w_down):
    def _A_ref(x, wqkv, wo):
        B, S, D = x.shape
        H = 32; Dh = D // H
        qkv = (lambda x, *d: x.permute(*d))((x @ wqkv).reshape(B, S, 3, H, Dh), 2, 0, 3, 1, 4)
        q, k, v = qkv[0], qkv[1], qkv[2]
        phi = lambda t: torch.where(t > 0, t + 1, torch.exp(t)) + 1e-6
        pq, pk = phi(q), phi(k)
        kv = torch.einsum("bhsd,bhse->bhde", pk, v)
        z = torch.einsum("bhsd->bhd", pk)
        num = torch.einsum("bhqd,bhde->bhqe", pq, kv)
        den = torch.einsum("bhqd,bhd->bhq", pq, z)[..., None] + 1e-6
        o = num / den
        return (lambda x, *d: x.permute(*d))(o, 0, 2, 1, 3).reshape(B, S, D) @ wo

    def _B_ref(x, w_gate, w_up, w_down):
        g = torch.einsum("bsd,df->bsf", x, w_gate)
        u = torch.einsum("bsd,df->bsf", x, w_up)
        h = _gelu(g) * u
        return torch.einsum("bsf,fd->bsd", h, w_down)

    x = A_x
    xn = _layernorm(x, axis=-1)
    s = _A_ref(xn, A_wqkv, A_wo)
    g = _silu(_B_ref(xn, B_w_gate, B_w_up, B_w_down))
    return x + s * g
```

```cpp
#include <hip/hip_runtime.h>
#include <hip/hip_bf16.h>
#include <stdint.h>

typedef unsigned short u16;
__device__ __forceinline__ float bf2f(u16 v){ return __uint_as_float(((uint32_t)v)<<16); }
__device__ __forceinline__ u16 f2bf(float f){ __hip_bfloat16 b = __float2bfloat16(f); return *(u16*)&b; }
__device__ __forceinline__ float rbf(float f){ return bf2f(f2bf(f)); }

constexpr int S=2048, D=512, F=1376, H=32, DH=16;

__global__ void k_ln(const u16* x, u16* xn){
  int row = blockIdx.x; int t = threadIdx.x;
  float v[8]; float s=0;
  for(int i=0;i<8;i++){ v[i]=bf2f(x[row*D+t*8+i]); s+=v[i]; }
  for(int o=32;o;o>>=1) s+=__shfl_xor(s,o);
  float m=s/D; float q=0;
  for(int i=0;i<8;i++){ float d=v[i]-m; q+=d*d; }
  for(int o=32;o;o>>=1) q+=__shfl_xor(q,o);
  float var=q/D;
  float r = 1.0f/sqrtf(var+1e-5f);
  for(int i=0;i<8;i++) xn[row*D+t*8+i]=f2bf((v[i]-m)*r);
}
__global__ void k_gemm(const u16* A, const u16* B, float* C, int M, int N, int K){
  __shared__ float As[16][17], Bs[16][17];
  int tx=threadIdx.x&15, ty=threadIdx.x>>4;
  int row=blockIdx.y*16+ty, col=blockIdx.x*16+tx;
  float acc=0;
  for(int k0=0;k0<K;k0+=16){
    As[ty][tx]=bf2f(A[row*K+k0+tx]);
    Bs[ty][tx]=bf2f(B[(k0+ty)*N+col]);
    __syncthreads();
    for(int k=0;k<16;k++) acc+=As[ty][k]*Bs[k][tx];
    __syncthreads();
  }
  C[row*N+col]=acc;
}
__device__ __forceinline__ float phi(float t){ return (t>0? t+1.0f : __expf(t)) + 1e-6f; }
__global__ void k_kv(const float* qkv, float* kv, float* z){
  int h=blockIdx.x; int d=threadIdx.x>>4, e=threadIdx.x&15;
  float a=0, zz=0;
  for(int s=0;s<S;s++){
    float pk=rbf(phi(rbf(qkv[s*1536+512+h*16+d])));
    float v=rbf(qkv[s*1536+1024+h*16+e]);
    a+=pk*v; zz+=pk;
  }
  kv[h*256+d*16+e]=a; if(e==0) z[h*16+d]=zz;
}
__global__ void k_o(const float* qkv, const float* kv, const float* z, u16* o){
  int s=blockIdx.x; int c=threadIdx.x; int h=c>>4, e=c&15;
  float num=0, den=0;
  for(int d=0;d<16;d++){
    float pq=rbf(phi(rbf(qkv[s*1536+h*16+d])));
    num+=pq*rbf(kv[h*256+d*16+e]); den+=pq*rbf(z[h*16+d]);
  }
  o[s*D+c]=f2bf(num/(den+1e-6f));
}
__global__ void k_h(const float* g, const float* u, u16* h){
  int i=blockIdx.x*256+threadIdx.x;
  float x=g[i];
  float t=tanhf(0.7978845608028654f*(x+0.044715f*x*x*x));
  h[i]=f2bf(0.5f*x*(1.0f+t)*u[i]);
}
__global__ void k_out(const u16* x, const float* s, const float* y, u16* out){
  int i=blockIdx.x*256+threadIdx.x;
  float yy=y[i]; float g=yy/(1.0f+__expf(-yy));
  out[i]=f2bf(bf2f(x[i])+s[i]*g);
}

extern "C" void kernel_launch(void* const* d_in, const int* in_sizes, int n_in,
                              void* d_out, int out_size, void* d_ws, size_t ws_size,
                              hipStream_t stream) {
  const u16* x=(const u16*)d_in[0]; const u16* wqkv=(const u16*)d_in[1]; const u16* wo=(const u16*)d_in[2];
  const u16* wg=(const u16*)d_in[4]; const u16* wu=(const u16*)d_in[5]; const u16* wd=(const u16*)d_in[6];
  char* ws=(char*)d_ws;
  u16* xn=(u16*)ws; ws+=S*D*2;
  float* qkv=(float*)ws; ws+=S*1536*4;
  float* g=(float*)ws; ws+=S*F*4;
  float* u=(float*)ws; ws+=S*F*4;
  u16* h=(u16*)ws; ws+=S*F*2;
  u16* o=(u16*)ws; ws+=S*D*2;
  float* kv=(float*)ws; ws+=H*256*4;
  float* z=(float*)ws; ws+=H*16*4;
  float* s=(float*)ws; ws+=S*D*4;
  float* y=(float*)ws; ws+=S*D*4;
  k_ln<<<S,64,0,stream>>>(x,xn);
  k_gemm<<<dim3(1536/16,S/16),256,0,stream>>>(xn,wqkv,qkv,S,1536,D);
  k_gemm<<<dim3(F/16,S/16),256,0,stream>>>(xn,wg,g,S,F,D);
  k_gemm<<<dim3(F/16,S/16),256,0,stream>>>(xn,wu,u,S,F,D);
  k_kv<<<H,256,0,stream>>>(qkv,kv,z);
  k_o<<<S,512,0,stream>>>(qkv,kv,z,o);
  k_h<<<S*F/256,256,0,stream>>>(g,u,h);
  k_gemm<<<dim3(D/16,S/16),256,0,stream>>>(o,wo,s,S,D,D);
  k_gemm<<<dim3(D/16,S/16),256,0,stream>>>(h,wd,y,S,D,F);
  k_out<<<S*D/256,256,0,stream>>>(x,s,y,(u16*)d_out);
}
```

```cpp
#include <hip/hip_runtime.h>
#include <hip/hip_bf16.h>
#include <stdint.h>

typedef unsigned short u16;
__device__ __forceinline__ float bf2f(u16 v){ return __uint_as_float(((uint32_t)v)<<16); }
__device__ __forceinline__ u16 f2bf(float f){ __hip_bfloat16 b = __float2bfloat16(f); return *(u16*)&b; }
__device__ __forceinline__ float rbf(float f){ return bf2f(f2bf(f)); }

constexpr int S=2048, D=512, F=1376, H=32, DH=16;

__global__ void k_ln(const u16* x, u16* xn){
  int row = blockIdx.x; int t = threadIdx.x;
  float v[8]; float s=0;
  for(int i=0;i<8;i++){ v[i]=bf2f(x[row*D+t*8+i]); s+=v[i]; }
  for(int o=32;o;o>>=1) s+=__shfl_xor(s,o);
  float m=s/D; float q=0;
  for(int i=0;i<8;i++){ float d=v[i]-m; q+=d*d; }
  for(int o=32;o;o>>=1) q+=__shfl_xor(q,o);
  float var=q/D;
  float r = 1.0f/sqrtf(var+1e-5f);
  for(int i=0;i<8;i++) xn[row*D+t*8+i]=f2bf((v[i]-m)*r);
}
__global__ void k_gemm(const u16* A, const u16* B, float* C, int M, int N, int K){
  __shared__ float As[16][17], Bs[16][17];
  int tx=threadIdx.x&15, ty=threadIdx.x>>4;
  int row=blockIdx.y*16+ty, col=blockIdx.x*16+tx;
  float acc=0;
  for(int k0=0;k0<K;k0+=16){
    As[ty][tx]=bf2f(A[row*K+k0+tx]);
    Bs[ty][tx]=bf2f(B[(k0+ty)*N+col]);
    __syncthreads();
    for(int k=0;k<16;k++) acc+=As[ty][k]*Bs[k][tx];
    __syncthreads();
  }
  C[row*N+col]=acc;
}
__device__ __forceinline__ float phi(float t){ return (t>0? t+1.0f : __expf(t)) + 1e-6f; }
__global__ void k_kv(const float* qkv, float* kv, float* z){
  int h=blockIdx.x; int d=threadIdx.x>>4, e=threadIdx.x&15;
  float a=0, zz=0;
  for(int s=0;s<S;s++){
    float pk=rbf(phi(rbf(qkv[s*1536+512+h*16+d])));
    float v=rbf(qkv[s*1536+1024+h*16+e]);
    a+=pk*v; zz+=pk;
  }
  kv[h*256+d*16+e]=a; if(e==0) z[h*16+d]=zz;
}
__global__ void k_o(const float* qkv, const float* kv, const float* z, u16* o){
  int s=blockIdx.x; int c=threadIdx.x; int h=c>>4, e=c&15;
  float num=0, den=0;
  for(int d=0;d<16;d++){
    float pq=rbf(phi(rbf(qkv[s*1536+h*16+d])));
    num+=pq*rbf(kv[h*256+d*16+e]); den+=pq*rbf(z[h*16+d]);
  }
  o[s*D+c]=f2bf(num/(den+1e-6f));
}
__global__ void k_h(const float* g, const float* u, u16* h){
  int i=blockIdx.x*256+threadIdx.x;
  float x=g[i];
  float t=tanhf(0.7978845608028654f*(x+0.044715f*x*x*x));
  h[i]=f2bf(0.5f*x*(1.0f+t)*u[i]);
}
__global__ void k_out(const u16* x, const float* s, const float* y, u16* out){
  int i=blockIdx.x*256+threadIdx.x;
  float yy=y[i]; float g=yy/(1.0f+__expf(-yy));
  out[i]=f2bf(bf2f(x[i])+s[i]*g);
}

typedef __attribute__((address_space(1))) const void* gptr;
typedef __attribute__((address_space(3))) void* lptr;
typedef __attribute__((ext_vector_type(4))) unsigned u32x4;

template<int INFL>
__global__ __launch_bounds__(512) void k_dma(const char* x, const char* wqkv, const char* wo, const char* wg, const char* wu, const char* wd, char* act, unsigned long long* tout, int mapmode, int dostore){
  extern __shared__ __attribute__((aligned(16))) char smem[];
  unsigned long long r0=__builtin_amdgcn_s_memrealtime();
  unsigned long long c0=__builtin_amdgcn_s_memtime();
  int lane=threadIdx.x&63; int w=__builtin_amdgcn_readfirstlane(threadIdx.x>>6);
  int b=blockIdx.x, xcd=b&7, i=b>>3;
  int p,G,jj;
  if(mapmode==0){ p=8*(xcd&1)+(i&7); G=xcd>>1; jj=i>>3; }
  else if(mapmode==1){ p=4*(xcd&3)+(i&3); G=(xcd>>2)*2+((i>>2)&1); jj=i>>3; }
  else { p=2*xcd+(i&1); G=(i>>1)&3; jj=i>>3; }
  bool kvt = jj<2; int hg=2*G+(jj&1);
  int slot=0;
  unsigned long long st[6];
  #define DMA(ptr) do{ __builtin_amdgcn_global_load_lds((gptr)(ptr),(lptr)(smem+(slot*8+w)*1024),16,0,0); slot=(slot+1==19)?0:slot+1; asm volatile("s_waitcnt vmcnt(%0)"::"n"(INFL):"memory"); }while(0)
  const char* xr0 = x + (size_t)(p*128 + w*16 + (lane>>3))*1024 + (lane&7)*16;
  const char* wgu = (w<4? wg: wu);
  bool first=true;
  if(kvt){
    const char* kv = wqkv + (size_t)(w*4 + (lane>>4))*3072 + 1024 + ((lane>>3)&1)*1024 + hg*128 + (lane&7)*16;
    for(int c=0;c<8;c++){
      DMA(xr0 + c*128); DMA(xr0+8*1024+c*128);
      DMA(kv + (size_t)(c*64)*3072); DMA(kv + (size_t)(c*64+32)*3072);
      if(c==4) st[0]=__builtin_amdgcn_s_memtime();
    }
    st[1]=__builtin_amdgcn_s_memtime();
    int f0 = hg<6? 768+80*hg : (hg==6?1232:1296);
    for(int r=0;r<20;r++){
      int ch = r*256 + (w&3)*64 + lane; int row=ch/10, cc=ch%10;
      DMA(wgu + (size_t)row*2752 + f0*2 + cc*16);
    }
  } else {
    int f0=96*hg;
    for(int c=0;c<8;c++){
      DMA(xr0 + c*128); DMA(xr0+8*1024+c*128);
      for(int r=0;r<3;r++){ int ch=(c*3+r)*256+(w&3)*64+lane; int row=ch/12, cc=ch%12; DMA(wgu + (size_t)row*2752 + f0*2 + cc*16); }
      if(c==3) st[0]=__builtin_amdgcn_s_memtime();
    }
    st[1]=__builtin_amdgcn_s_memtime();
    if(dostore){
      u32x4 v={1,2,3,4};
      for(int u=0;u<3;u++){
        char* dst = act + ((size_t)(p*4+(w&3))*118 + (f0/16 + (w>>2)*3+u))*1024 + lane*16;
        asm volatile("global_store_dwordx4 %0, %1, off sc1\n\ts_nop 1"::"v"(dst),"v"(v):"memory");
      }
    }
    const char* q = wqkv + (size_t)(w*8 + (lane>>3))*3072 + hg*128 + (lane&7)*16;
    for(int c=0;c<8;c++) DMA(q + (size_t)(c*64)*3072);
    const char* part = act + (8u<<20) + hg*73728 + w*1024 + lane*16;
    for(int c=0;c<9;c++) DMA(part + c*8192);
  }
  st[2]=__builtin_amdgcn_s_memtime();
  if(dostore){
    u32x4 v={1,2,3,4};
    int n = kvt? ((w>>2)?2:3) : 2;
    int f0 = hg<6? 768+80*hg : (hg==6?1232:1296);
    for(int u=0;u<n;u++){
      int step = kvt? (f0/16 + (w>>2)*3+u) : (86 + hg*4 + (w>>2)*2+u);
      char* dst = act + ((size_t)(p*4+(w&3))*118 + step)*1024 + lane*16;
      asm volatile("global_store_dwordx4 %0, %1, off sc1\n\ts_nop 1"::"v"(dst),"v"(v):"memory");
    }
  }
  int rh, cb;
  if(mapmode==0){ rh=G&1; cb=4*(G>>1)+jj; }
  else if(mapmode==1){ rh=G&1; cb=4*(G>>1)+jj; }
  else { rh=G&1; cb=4*(G>>1)+jj; }
  const char* bd = wd + (size_t)(w*8+(lane>>3))*1024 + cb*128 + (lane&7)*16;
  const char* bo = wo + (size_t)(w*8+(lane>>3))*1024 + cb*128 + (lane&7)*16;
  const char* a0 = act + ((size_t)(p*4+rh*2+(w&1))*118 + (w>>1))*1024 + lane*16;
  for(int r=0;r<12;r++) DMA(bd + (size_t)r*65536);
  st[3]=__builtin_amdgcn_s_memtime();
  for(int r=0;r<12;r++) DMA(a0 + r*4096);
  for(int r=12;r<21;r++){ DMA(bd + (size_t)r*65536); DMA(a0 + r*4096); }
  {
    const char* ptr = (w<4)? bd + (size_t)21*65536 : act + ((size_t)(p*4+rh*2+(w&1))*118 + 84 + ((w>>1)&1))*1024 + lane*16;
    DMA(ptr);
  }
  for(int r=0;r<8;r++){ DMA(bo + (size_t)r*65536); DMA(a0 + 86*1024 + r*4096); }
  asm volatile("s_waitcnt vmcnt(0)":::"memory");
  __syncthreads();
  st[4]=__builtin_amdgcn_s_memtime();
  unsigned long long r1=__builtin_amdgcn_s_memrealtime();
  if(threadIdx.x==0){ unsigned long long* o=tout+b*8; o[0]=r0; o[1]=r1; for(int k=0;k<5;k++) o[2+k]=st[k]-c0; o[7]=kvt; }
}

__global__ void k_spin(const unsigned long long* t, int metric){
  unsigned long long mn=~0ull,mx=0,mxs=0; unsigned long long acc=0;
  for(int b=0;b<256;b++){ unsigned long long a=t[b*8], e=t[b*8+1]; mn=a<mn?a:mn; mx=e>mx?e:mx; mxs=a>mxs?a:mxs; acc+=t[b*8+2+(metric>=2?metric-2:0)]; }
  unsigned long long m = metric==0? (mx-mn) : metric==1? (mxs-mn) : acc/256/10;
  if(m>5000) m=5000;
  unsigned long long r0=__builtin_amdgcn_s_memrealtime();
  while(__builtin_amdgcn_s_memrealtime()-r0 < m*100) __builtin_amdgcn_s_sleep(8);
}
extern "C" void kernel_launch(void* const* d_in, const int* in_sizes, int n_in,
                              void* d_out, int out_size, void* d_ws, size_t ws_size,
                              hipStream_t stream) {
  const u16* x=(const u16*)d_in[0]; const u16* wqkv=(const u16*)d_in[1]; const u16* wo=(const u16*)d_in[2];
  const u16* wg=(const u16*)d_in[4]; const u16* wu=(const u16*)d_in[5]; const u16* wd=(const u16*)d_in[6];
  char* ws=(char*)d_ws;
  u16* xn=(u16*)ws; ws+=S*D*2;
  float* qkv=(float*)ws; ws+=S*1536*4;
  float* g=(float*)ws; ws+=S*F*4;
  float* u=(float*)ws; ws+=S*F*4;
  u16* h=(u16*)ws; ws+=S*F*2;
  u16* o=(u16*)ws; ws+=S*D*2;
  float* kv=(float*)ws; ws+=H*256*4;
  float* z=(float*)ws; ws+=H*16*4;
  float* s=(float*)ws; ws+=S*D*4;
  float* y=(float*)ws; ws+=S*D*4;
  {
    hipFuncSetAttribute((const void*)k_dma<12>, hipFuncAttributeMaxDynamicSharedMemorySize, 19*8192);
    char* act=(char*)d_ws + (128u<<20); unsigned long long* tout=(unsigned long long*)((char*)d_ws + (200u<<20));
    k_dma<12><<<256,512,19*8192,stream>>>((const char*)x,(const char*)wqkv,(const char*)wo,(const char*)wg,(const char*)wu,(const char*)wd,act,tout,0,1);
    k_spin<<<1,1,0,stream>>>(tout,1);
  }
  k_ln<<<S,64,0,stream>>>(x,xn);
  k_gemm<<<dim3(1536/16,S/16),256,0,stream>>>(xn,wqkv,qkv,S,1536,D);
  k_gemm<<<dim3(F/16,S/16),256,0,stream>>>(xn,wg,g,S,F,D);
  k_gemm<<<dim3(F/16,S/16),256,0,stream>>>(xn,wu,u,S,F,D);
  k_kv<<<H,256,0,stream>>>(qkv,kv,z);
  k_o<<<S,512,0,stream>>>(qkv,kv,z,o);
  k_h<<<S*F/256,256,0,stream>>>(g,u,h);
  k_gemm<<<dim3(D/16,S/16),256,0,stream>>>(o,wo,s,S,D,D);
  k_gemm<<<dim3(D/16,S/16),256,0,stream>>>(h,wd,y,S,D,F);
  k_out<<<S*D/256,256,0,stream>>>(x,s,y,(u16*)d_out);
}
```

```cpp
#include <hip/hip_runtime.h>

__global__ __launch_bounds__(512, 2) void fused_block_kernel(const void* x, const void* wqkv, const void* wo, const void* wg,
                                             const void* wu, const void* wd, void* out, void* ws) {
  asm volatile(
".Lfb_KSTART:\n"
"s_load_dwordx16 s[4:19], s[0:1], 0x0\n"
"v_readfirstlane_b32 s3, v0\n"
"v_and_b32 v0, 63, v0\n"
"s_and_b32 s24, s2, 1\n"
"s_bfe_u32 s25, s2, 0x20001\n"
"s_lshr_b32 s26, s2, 3\n"
"s_lshr_b32 s3, s3, 6\n"
"s_and_b32 s20, s3, 3\n"
"s_cmp_lt_u32 s3, 4\n"
"s_cbranch_scc1 .Lfb_C_EARLY\n"
"s_mul_i32 s22, s26, 52\n"
"s_lshr_b32 s22, s22, 8\n"
"s_mul_i32 s34, s22, 5\n"
"s_sub_u32 s27, s26, s34\n"
"s_mul_i32 s21, s24, 5\n"
"s_add_u32 s21, s21, s27\n"
"s_mov_b32 s23, s25\n"
"s_mov_b32 s28, 0\n"
"s_cmp_eq_u32 s25, 3\n"
"s_cselect_b32 s34, 25, 30\n"
"s_cmp_lt_u32 s26, s34\n"
"s_cbranch_scc0 .Lfb_MAPX\n"
".Lfb_MAPDH:\n"
"s_add_u32 s47, s27, s22\n"
"s_and_b32 s47, s47, 3\n"
"s_mov_b32 s46, 0x76543120\n"
"s_bitcmp1_b32 s47, 0\n"
"s_cselect_b32 s46, 0x67452031, s46\n"
"s_lshl1_add_u32 s34, s24, s25\n"
"s_and_b32 s34, s34, 3\n"
"s_mul_i32 s34, s34, 0x22222222\n"
"s_add_u32 s46, s46, s34\n"
"v_lshrrev_b32 v8, 3, v0\n"
"v_lshrrev_b32 v9, 4, v0\n"
"v_and_b32 v10, 7, v0\n"
"s_lshl_b32 s55, s20, 10\n"
"v_lshl_add_u32 v11, s20, 2, v9\n"
"v_lshl_add_u32 v12, s20, 3, v8\n"
"v_and_b32 v11, 7, v11\n"
"v_xor_b32 v11, v11, v10\n"
"v_lshlrev_b32_e64 v11, 4, v11\n"
"v_lshl_add_u32 v16, v12, 10, v11\n"
"s_mul_i32 s36, s21, 196608\n"
"s_and_b32 s34, s46, 7\n"
"s_lshl_b32 s34, s34, 7\n"
"s_cmp_eq_u32 s21, 10\n"
"s_cselect_b32 s37, 0, -1\n"
"s_cmp_lt_u32 s22, 2\n"
"s_cbranch_scc1 .Lfb_H_Q\n"
"s_lshl2_add_u32 s62, s23, s22\n"
"s_sub_u32 s62, s62, 2\n"
"s_cmp_eq_u32 s62, 14\n"
"s_cselect_b64 vcc, -1, 0\n"
"s_mul_i32 s62, s62, 192\n"
"s_mov_b32 s63, s62\n"
"s_mov_b32 s54, 176128\n"
"s_mov_b32 s35, 2752\n"
"v_lshl_add_u32 v13, s20, 6, v0\n"
"v_and_b32 v29, 1, v0\n"
"v_mul_u32_u24 v14, 43691, v13\n"
"v_lshrrev_b32 v14, 19, v14\n"
"v_mul_u32_u24 v11, 12, v14\n"
"v_sub_u32 v11, v13, v11\n"
"v_lshrrev_b32 v28, 2, v14\n"
"v_and_b32 v28, 2, v28\n"
"v_xor_b32 v11, v11, v28\n"
"v_lshrrev_b32_e64 v28, 2, v11\n"
"v_and_or_b32 v28, v28, 2, v29\n"
"v_cndmask_b32 v11, v11, v28, vcc\n"
"v_lshlrev_b32 v11, 4, v11\n"
"v_mad_u32_u24 v22, v14, s35, v11\n"
"v_add_u32 v13, 256, v13\n"
"v_mul_u32_u24 v14, 43691, v13\n"
"v_lshrrev_b32 v14, 19, v14\n"
"v_mul_u32_u24 v11, 12, v14\n"
"v_sub_u32 v11, v13, v11\n"
"v_lshrrev_b32 v28, 2, v14\n"
"v_and_b32 v28, 2, v28\n"
"v_xor_b32 v11, v11, v28\n"
"v_lshrrev_b32_e64 v28, 2, v11\n"
"v_and_or_b32 v28, v28, 2, v29\n"
"v_cndmask_b32 v11, v11, v28, vcc\n"
"v_lshlrev_b32 v11, 4, v11\n"
"v_mad_u32_u24 v23, v14, s35, v11\n"
"v_add_u32 v13, 256, v13\n"
"v_mul_u32_u24 v14, 43691, v13\n"
"v_lshrrev_b32 v14, 19, v14\n"
"v_mul_u32_u24 v11, 12, v14\n"
"v_sub_u32 v11, v13, v11\n"
"v_lshrrev_b32 v28, 2, v14\n"
"v_and_b32 v28, 2, v28\n"
"v_xor_b32 v11, v11, v28\n"
"v_lshrrev_b32_e64 v28, 2, v11\n"
"v_and_or_b32 v28, v28, 2, v29\n"
"v_cndmask_b32 v11, v11, v28, vcc\n"
"v_lshlrev_b32 v11, 4, v11\n"
"v_mad_u32_u24 v24, v14, s35, v11\n"
"s_branch .Lfb_H_W\n"
".Lfb_H_Q:\n"
"s_lshl1_add_u32 s62, s23, s22\n"
"s_lshl_b32 s62, s62, 7\n"
"s_add_u32 s63, s62, 98304\n"
"s_mov_b32 s54, 196608\n"
"s_mov_b32 s35, 3072\n"
"s_and_b32 s38, s20, 1\n"
"s_lshl_b32 s38, s38, 2\n"
"v_and_b32 v11, 1, v9\n"
"v_lshl_or_b32 v11, v11, 1, s38\n"
"v_xor_b32 v11, v11, v10\n"
"v_lshlrev_b32 v11, 4, v11\n"
"v_mad_u32_u24 v22, v12, s35, v11\n"
"v_add_u32 v23, 1024, v22\n"
"v_add_u32 v24, 2048, v22\n"
".Lfb_H_W:\n"
"s_cmp_eq_u32 s28, 1\n"
"s_waitcnt lgkmcnt(0)\n"
"s_cbranch_scc1 .Lfb_H_IDLE\n"
"s_add_u32 s48, s4, s36\n"
"s_addc_u32 s49, s5, 0\n"
"s_add_u32 s56, s48, s34\n"
"s_addc_u32 s57, s49, 0\n"
"s_add_u32 m0, s55, 0\n"
"v_add_u32 v17, 0x8000, v16\n"
"global_load_lds_dwordx4 v16, s[56:57]\n"
"s_add_u32 m0, s55, 4096\n"
"v_add_u32 v18, 0x10000, v16\n"
"global_load_lds_dwordx4 v17, s[56:57]\n"
"s_add_u32 m0, s55, 8192\n"
"v_add_u32 v19, 0x18000, v16\n"
"global_load_lds_dwordx4 v18, s[56:57]\n"
"s_add_u32 m0, s55, 12288\n"
"v_add_u32 v20, 0x20000, v16\n"
"global_load_lds_dwordx4 v19, s[56:57]\n"
"s_add_u32 m0, s55, 16384\n"
"v_add_u32 v21, 0x28000, v16\n"
"v_and_b32_e64 v20, s37, v20\n"
"global_load_lds_dwordx4 v20, s[56:57]\n"
"s_add_u32 m0, s55, 20480\n"
"v_and_b32_e64 v21, s37, v21\n"
"global_load_lds_dwordx4 v21, s[56:57]\n"
"s_load_dwordx2 s[32:33], s[18:19], 0x0\n"
"s_cmp_lt_u32 s22, 2\n"
"s_cselect_b64 s[50:51], s[6:7], s[10:11]\n"
"s_cselect_b64 s[52:53], s[6:7], s[12:13]\n"
"s_add_u32 s50, s50, s62\n"
"s_addc_u32 s51, s51, 0\n"
"s_add_u32 s52, s52, s63\n"
"s_addc_u32 s53, s53, 0\n"
"s_mov_b32 s78, s46\n"
"s_and_b32 s34, s46, 7\n"
"s_lshr_b32 s46, s46, 4\n"
"s_lshl_b32 s35, s34, 7\n"
"s_mul_i32 s34, s34, s54\n"
"s_add_u32 s56, s48, s35\n"
"s_addc_u32 s57, s49, 0\n"
"s_add_u32 s58, s50, s34\n"
"s_addc_u32 s59, s51, 0\n"
"s_add_u32 s60, s52, s34\n"
"s_addc_u32 s61, s53, 0\n"
"s_add_u32 m0, s55, 24576\n"
"s_nop 0\n"
"global_load_lds_dwordx4 v22, s[58:59]\n"
"s_add_u32 m0, s55, 36864\n"
"s_nop 0\n"
"global_load_lds_dwordx4 v22, s[60:61]\n"
"s_add_u32 m0, s55, 28672\n"
"s_nop 0\n"
"global_load_lds_dwordx4 v23, s[58:59]\n"
"s_add_u32 m0, s55, 40960\n"
"s_nop 0\n"
"global_load_lds_dwordx4 v23, s[60:61]\n"
"s_add_u32 m0, s55, 32768\n"
"s_nop 0\n"
"global_load_lds_dwordx4 v24, s[58:59]\n"
"s_add_u32 m0, s55, 45056\n"
"s_nop 0\n"
"global_load_lds_dwordx4 v24, s[60:61]\n"
"s_branch .Lfb_H_S2\n"
".Lfb_MAPX:\n"
"s_sub_u32 s34, s26, s34\n"
"s_mov_b32 s21, 10\n"
"s_mov_b32 s27, 5\n"
"s_lshl1_add_u32 s22, s24, s34\n"
"s_cmp_lt_u32 s25, 3\n"
"s_cbranch_scc1 .Lfb_MAPR\n"
"s_mov_b32 s35, 0xf765210\n"
"s_cmp_eq_u32 s24, 1\n"
"s_cselect_b32 s35, 0xffa9843, s35\n"
"s_lshl_b32 s34, s34, 2\n"
"s_lshr_b32 s35, s35, s34\n"
"s_and_b32 s22, s35, 15\n"
"s_cmp_lt_u32 s22, 5\n"
"s_cbranch_scc1 .Lfb_MAPR\n"
"s_cmp_eq_u32 s22, 15\n"
"s_cselect_b32 s28, 1, 0\n"
"s_sub_u32 s34, s22, 5\n"
"s_lshr_b32 s23, s34, 1\n"
"s_and_b32 s22, s34, 1\n"
"s_add_u32 s22, s22, 4\n"
".Lfb_MAPR:\n"
"s_cmp_ge_u32 s3, 4\n"
"s_cbranch_scc1 .Lfb_MAPDH\n"
"s_branch .Lfb_MAPDC\n"
".Lfb_C_EARLY:\n"
"s_getpc_b64 s[36:37]\n"
".Lfb_PCREF:\n"
"s_lshl2_add_u32 s34, s26, s20\n"
"s_mul_i32 s34, s34, 384\n"
"v_lshl_add_u32 v15, v0, 7, s34\n"
"v_min_u32 v15, (.Lfb_KEND-.Lfb_PCREF-4), v15\n"
"s_mov_b64 exec, 7\n"
"global_load_dword v255, v15, s[36:37]\n"
"s_mov_b64 exec, -1\n"
"s_lshl2_add_u32 s34, s24, s25\n"
"s_lshl_b32 s34, s34, 5\n"
"s_add_u32 s34, s34, s26\n"
"s_lshl2_add_u32 s34, s34, s20\n"
"s_lshl_b32 s34, s34, 11\n"
"v_and_b32 v60, 31, v0\n"
"v_lshrrev_b32 v61, 5, v0\n"
"v_lshlrev_b32 v60, 4, v60\n"
"v_lshl_add_u32 v60, v61, 10, v60\n"
"s_waitcnt lgkmcnt(0)\n"
"s_add_u32 s36, s4, s34\n"
"s_addc_u32 s37, s5, 0\n"
"global_load_dwordx4 v[32:35], v60, s[36:37]\n"
"global_load_dwordx4 v[36:39], v60, s[36:37] offset:512\n"
"s_cmp_eq_u32 s20, 0\n"
"s_cselect_b64 s[36:37], s[4:5], s[6:7]\n"
"s_cselect_b32 s34, 30, 16\n"
"s_cselect_b32 s35, 1, 3\n"
"s_cselect_b32 s38, s24, 0\n"
"s_bfm_b64 s[40:41], s34, 0\n"
"s_lshl_b32 s38, s38, 5\n"
"s_add_u32 s38, s38, s26\n"
"s_mul_i32 s34, s34, s38\n"
"v_add_u32 v62, s34, v0\n"
"v_mul_u32_u24 v62, s35, v62\n"
"s_sub_u32 s34, 0x1ff, s36\n"
"s_and_b32 s34, s34, 0x380\n"
"s_max_u32 s35, s20, 1\n"
"s_sub_u32 s35, s35, 1\n"
"v_add_u32_e64 v62, s35, v62\n"
"v_lshl_add_u32 v62, v62, 10, s34\n"
"s_lshr_b32 s34, s34, 8\n"
"s_cmp_eq_u32 s34, s25\n"
"s_cselect_b32 s34, 14, 0\n"
"s_or_b32 s34, s34, 0\n"
"s_bitcmp1_b32 s34, s20\n"
"s_cselect_b64 exec, s[40:41], 1\n"
"global_load_dword v15, v62, s[36:37]\n"
"s_mov_b64 exec, -1\n"
"s_mul_i32 s22, s26, 52\n"
"s_lshr_b32 s22, s22, 8\n"
"s_mul_i32 s34, s22, 5\n"
"s_sub_u32 s27, s26, s34\n"
"s_mul_i32 s21, s24, 5\n"
"s_add_u32 s21, s21, s27\n"
"s_mov_b32 s23, s25\n"
"s_mov_b32 s28, 0\n"
"s_cmp_eq_u32 s25, 3\n"
"s_cselect_b32 s34, 25, 30\n"
"s_cmp_lt_u32 s26, s34\n"
"s_cbranch_scc0 .Lfb_MAPX\n"
".Lfb_MAPDC:\n"
"s_branch .Lfb_C_START\n"
".Lfb_H_S2:\n"
"s_and_b32 s34, s46, 7\n"
"s_lshr_b32 s46, s46, 4\n"
"s_lshl_b32 s35, s34, 7\n"
"s_mul_i32 s34, s34, s54\n"
"s_add_u32 s56, s48, s35\n"
"s_addc_u32 s57, s49, 0\n"
"s_add_u32 s58, s50, s34\n"
"s_addc_u32 s59, s51, 0\n"
"s_add_u32 s60, s52, s34\n"
"s_addc_u32 s61, s53, 0\n"
"s_lshl_b32 s47, s47, 2\n"
"s_lshr_b32 s47, 0x1100, s47\n"
"s_and_b32 s47, s47, 15\n"
"s_lshl_b32 s34, s47, 2\n"
"s_lshr_b32 s78, s78, s34\n"
"s_and_b32 s78, s78, 7\n"
"s_cmp_lt_u32 s27, 4\n"
"s_cselect_b32 s47, s47, 15\n"
"v_bfe_u32 v92, v0, 2, 2\n"
"v_lshrrev_b32 v97, 4, v0\n"
"v_lshl_add_u32 v98, v97, 3, v92\n"
"v_and_b32 v99, 3, v0\n"
"v_and_b32 v100, 1, v97\n"
"s_cmp_eq_u32 s47, 3\n"
"s_cselect_b32 s38, 0, s47\n"
"s_mul_i32 s38, s38, 49152\n"
"s_add_u32 s38, s38, 24576\n"
"s_lshr_b32 s34, s20, 1\n"
"s_and_b32 s35, s20, 1\n"
"s_cmp_lt_u32 s22, 2\n"
"s_cbranch_scc1 .Lfb_H_CSQ\n"
"s_mul_i32 s34, s34, 3\n"
"s_mul_i32 s35, s35, 12288\n"
"s_add_u32 s38, s38, s35\n"
"s_mov_b32 s62, 6144\n"
"s_mov_b32 s63, 768\n"
"v_mul_u32_u24 v98, 192, v98\n"
"v_lshl_add_u32 v98, v99, 3, v98\n"
"v_add_u32 v98, s38, v98\n"
"v_xor_b32 v101, s34, v100\n"
"v_lshl_add_u32 v25, v101, 5, v98\n"
"s_add_u32 s34, s34, 1\n"
"v_xor_b32 v101, s34, v100\n"
"v_lshl_add_u32 v26, v101, 5, v98\n"
"s_add_u32 s34, s34, 1\n"
"v_xor_b32 v101, s34, v100\n"
"v_lshl_add_u32 v27, v101, 5, v98\n"
"s_branch .Lfb_H_CSR\n"
".Lfb_H_CSQ:\n"
"s_mov_b32 s62, 12288\n"
"s_mov_b32 s63, 512\n"
"v_lshrrev_b32 v92, 1, v92\n"
"v_lshl_or_b32 v100, v100, 1, v92\n"
"v_lshlrev_b32_e64 v98, 7, v98\n"
"v_lshl_add_u32 v98, v99, 3, v98\n"
"v_add_u32 v98, s38, v98\n"
"v_xor_b32 v101, s20, v100\n"
"v_lshl_add_u32 v25, v101, 5, v98\n"
"v_add_u32 v26, 0x1000, v25\n"
"v_add_u32 v27, 0x2000, v25\n"
"v_lshrrev_b32 v101, 1, v99\n"
"v_xor_b32 v101, v101, v99\n"
"v_and_b32 v101, 1, v101\n"
"v_mul_u32_u24 v101, 24, v101\n"
"v_xor_b32 v27, v101, v27\n"
".Lfb_H_CSR:\n"
"v_add_u32 v144, s63, v25\n"
"v_add_u32 v145, s62, v25\n"
"v_add_u32 v146, s62, v144\n"
"v_add_u32 v147, s63, v26\n"
"v_add_u32 v148, s62, v26\n"
"v_add_u32 v149, s62, v147\n"
"v_add_u32 v150, s63, v27\n"
"v_add_u32 v151, s62, v27\n"
"v_add_u32 v152, s62, v150\n"
"v_mov_b32 v128, 0x3f803f80\n"
"v_mov_b32 v129, 0x3f803f80\n"
"v_mov_b32 v130, 0x3f803f80\n"
"v_mov_b32 v131, 0x3f803f80\n"
"s_mul_i32 s34, s23, 6\n"
"s_add_u32 s34, s34, s22\n"
"s_lshl_b32 s34, s34, 13\n"
"s_add_u32 s34, s34, 9568256\n"
"s_add_u32 s64, s18, s34\n"
"s_addc_u32 s65, s19, 0\n"
"s_lshl2_add_u32 s35, s78, s20\n"
"s_lshl_b32 s35, s35, 8\n"
"s_add_u32 s34, s34, s35\n"
"v_lshl_add_u32 v96, v0, 4, s34\n"
"s_lshl_b32 s34, s20, 11\n"
"v_lshl_add_u32 v80, v0, 4, s34\n"
"v_add_u32 v81, 1024, v80\n"
"s_mul_i32 s34, s21, 6144\n"
"v_add_u32 v82, s34, v80\n"
"v_add_u32 v83, 1024, v82\n"
"v_min_u32 v82, 0xfff0, v82\n"
"v_min_u32 v83, 0xfff0, v83\n"
"s_mul_i32 s34, s20, 8\n"
"s_nop 0\n"
"s_add_u32 s34, s34, 147468\n"
"v_add_u32 v88, s34, v80\n"
"s_cmp_eq_u32 s20, 3\n"
"s_cselect_b32 s34, s34, 155692\n"
"v_add_u32 v89, s34, v80\n"
"s_waitcnt lgkmcnt(0)\n"
"s_add_u32 s32, s32, 0x9e3779b9\n"
"s_addc_u32 s33, s33, 0x7f4a7c15\n"
"v_mov_b32 v28, 161824\n"
"v_mov_b32 v44, s32\n"
"v_mov_b32 v45, s33\n"
"v_mov_b64 v[46:47], 0\n"
"v_mov_b64 v[48:49], 0\n"
"ds_write_b64 v28, v[44:45] offset:32\n"
"ds_write_b128 v28, v[46:49]\n"
"ds_write_b128 v28, v[46:49] offset:16\n"
"ds_write_b128 v28, v[46:49] offset:48\n"
"s_add_u32 s76, s18, 65536\n"
"s_addc_u32 s77, s19, 0\n"
"s_lshl2_add_u32 s34, s24, s25\n"
"s_lshl_b32 s34, s34, 5\n"
"s_add_u32 s34, s34, s26\n"
"s_lshl_b32 s34, s34, 8\n"
"v_lshlrev_b32 v31, 4, v0\n"
"v_add_u32_e64 v30, s34, v31\n"
"v_add_u32 v31, 161888, v31\n"
"v_mov_b32_e64 v103, s32\n"
"s_add_u32 m0, s55, 49152\n"
"s_nop 0\n"
"global_load_lds_dwordx4 v16, s[56:57]\n"
"s_add_u32 m0, s55, 73728\n"
"s_nop 0\n"
"global_load_lds_dwordx4 v22, s[58:59]\n"
"s_add_u32 m0, s55, 53248\n"
"s_nop 0\n"
"global_load_lds_dwordx4 v17, s[56:57]\n"
"s_add_u32 m0, s55, 86016\n"
"s_nop 0\n"
"global_load_lds_dwordx4 v22, s[60:61]\n"
"s_add_u32 m0, s55, 57344\n"
"s_nop 0\n"
"global_load_lds_dwordx4 v18, s[56:57]\n"
"s_add_u32 m0, s55, 77824\n"
"s_nop 0\n"
"global_load_lds_dwordx4 v23, s[58:59]\n"
"s_add_u32 m0, s55, 61440\n"
"s_nop 0\n"
"global_load_lds_dwordx4 v19, s[56:57]\n"
"s_add_u32 m0, s55, 90112\n"
"s_nop 0\n"
"global_load_lds_dwordx4 v23, s[60:61]\n"
"s_add_u32 m0, s55, 65536\n"
"s_nop 0\n"
"global_load_lds_dwordx4 v20, s[56:57]\n"
"s_add_u32 m0, s55, 81920\n"
"s_nop 0\n"
"global_load_lds_dwordx4 v24, s[58:59]\n"
"s_add_u32 m0, s55, 69632\n"
"s_nop 0\n"
"global_load_lds_dwordx4 v21, s[56:57]\n"
"s_add_u32 m0, s55, 94208\n"
"s_nop 0\n"
"global_load_lds_dwordx4 v24, s[60:61]\n"
"s_waitcnt lgkmcnt(0)\n"
"s_waitcnt vmcnt(12)\n"
"s_barrier\n"
"s_cmp_lg_u32 s47, 0\n"
"s_cbranch_scc1 .Lfb_H_CR0\n"
"s_nop 0\n"
"ds_read_b64_tr_b16 v[104:105], v25\n"
"ds_read_b64_tr_b16 v[106:107], v144\n"
"ds_read_b64_tr_b16 v[116:117], v145\n"
"ds_read_b64_tr_b16 v[118:119], v146\n"
"ds_read_b64_tr_b16 v[108:109], v26\n"
"ds_read_b64_tr_b16 v[110:111], v147\n"
"ds_read_b64_tr_b16 v[120:121], v148\n"
"ds_read_b64_tr_b16 v[122:123], v149\n"
"ds_read_b64_tr_b16 v[112:113], v27\n"
"ds_read_b64_tr_b16 v[114:115], v150\n"
"ds_read_b64_tr_b16 v[124:125], v151\n"
"ds_read_b64_tr_b16 v[126:127], v152\n"
".Lfb_H_CR0:\n"
"s_and_b32 s34, s46, 7\n"
"s_lshr_b32 s46, s46, 4\n"
"s_lshl_b32 s35, s34, 7\n"
"s_mul_i32 s34, s34, s54\n"
"s_add_u32 s56, s48, s35\n"
"s_addc_u32 s57, s49, 0\n"
"s_add_u32 s58, s50, s34\n"
"s_addc_u32 s59, s51, 0\n"
"s_add_u32 s60, s52, s34\n"
"s_addc_u32 s61, s53, 0\n"
"s_add_u32 m0, s55, 98304\n"
"s_nop 0\n"
"global_load_lds_dwordx4 v16, s[56:57]\n"
"s_add_u32 m0, s55, 122880\n"
"s_nop 0\n"
"global_load_lds_dwordx4 v22, s[58:59]\n"
"s_add_u32 m0, s55, 102400\n"
"s_nop 0\n"
"global_load_lds_dwordx4 v17, s[56:57]\n"
"s_add_u32 m0, s55, 135168\n"
"s_nop 0\n"
"global_load_lds_dwordx4 v22, s[60:61]\n"
"s_add_u32 m0, s55, 106496\n"
"s_nop 0\n"
"global_load_lds_dwordx4 v18, s[56:57]\n"
"s_add_u32 m0, s55, 126976\n"
"s_nop 0\n"
"global_load_lds_dwordx4 v23, s[58:59]\n"
"s_add_u32 m0, s55, 110592\n"
"s_nop 0\n"
"global_load_lds_dwordx4 v19, s[56:57]\n"
"s_add_u32 m0, s55, 139264\n"
"s_nop 0\n"
"global_load_lds_dwordx4 v23, s[60:61]\n"
"s_add_u32 m0, s55, 114688\n"
"s_nop 0\n"
"global_load_lds_dwordx4 v20, s[56:57]\n"
"s_add_u32 m0, s55, 131072\n"
"s_nop 0\n"
"global_load_lds_dwordx4 v24, s[58:59]\n"
"s_add_u32 m0, s55, 118784\n"
"s_nop 0\n"
"global_load_lds_dwordx4 v21, s[56:57]\n"
"s_add_u32 m0, s55, 143360\n"
"s_nop 0\n"
"global_load_lds_dwordx4 v24, s[60:61]\n"
"s_waitcnt lgkmcnt(0)\n"
"s_waitcnt vmcnt(12)\n"
"s_barrier\n"
"s_cmp_lg_u32 s47, 1\n"
"s_cbranch_scc1 .Lfb_H_CR1\n"
"s_nop 0\n"
"ds_read_b64_tr_b16 v[104:105], v25\n"
"ds_read_b64_tr_b16 v[106:107], v144\n"
"ds_read_b64_tr_b16 v[116:117], v145\n"
"ds_read_b64_tr_b16 v[118:119], v146\n"
"ds_read_b64_tr_b16 v[108:109], v26\n"
"ds_read_b64_tr_b16 v[110:111], v147\n"
"ds_read_b64_tr_b16 v[120:121], v148\n"
"ds_read_b64_tr_b16 v[122:123], v149\n"
"ds_read_b64_tr_b16 v[112:113], v27\n"
"ds_read_b64_tr_b16 v[114:115], v150\n"
"ds_read_b64_tr_b16 v[124:125], v151\n"
"ds_read_b64_tr_b16 v[126:127], v152\n"
".Lfb_H_CR1:\n"
"s_cmp_lg_u32 s20, 0\n"
"s_cbranch_scc1 .Lfb_PS_1\n"
"s_mov_b64 exec, 0xffff\n"
"ds_read_b128 v[40:43], v31\n"
"s_waitcnt lgkmcnt(0)\n"
"v_mov_b32 v43, s32\n"
"global_store_dwordx4 v30, v[40:43], s[76:77] sc1\n"
"s_mov_b64 exec, -1\n"
".Lfb_PS_1:\n"
"s_and_b32 s34, s46, 7\n"
"s_lshr_b32 s46, s46, 4\n"
"s_lshl_b32 s35, s34, 7\n"
"s_mul_i32 s34, s34, s54\n"
"s_add_u32 s56, s48, s35\n"
"s_addc_u32 s57, s49, 0\n"
"s_add_u32 s58, s50, s34\n"
"s_addc_u32 s59, s51, 0\n"
"s_add_u32 s60, s52, s34\n"
"s_addc_u32 s61, s53, 0\n"
"s_add_u32 m0, s55, 0\n"
"s_nop 0\n"
"global_load_lds_dwordx4 v16, s[56:57]\n"
"s_add_u32 m0, s55, 24576\n"
"s_nop 0\n"
"global_load_lds_dwordx4 v22, s[58:59]\n"
"s_add_u32 m0, s55, 4096\n"
"s_nop 0\n"
"s_nop 0\n"
"global_load_lds_dwordx4 v17, s[56:57]\n"
"s_waitcnt lgkmcnt(0)\n"
"s_setprio 1\n"
"v_mfma_f32_16x16x32_bf16 v[132:135], v[128:131], v[104:107], 0\n"
"v_mfma_f32_16x16x32_bf16 v[136:139], v[128:131], v[108:111], 0\n"
"v_mfma_f32_16x16x32_bf16 v[140:143], v[128:131], v[112:115], 0\n"
"v_mfma_f32_16x16x32_bf16 v[132:135], v[128:131], v[116:119], v[132:135]\n"
"v_mfma_f32_16x16x32_bf16 v[136:139], v[128:131], v[120:123], v[136:139]\n"
"v_mfma_f32_16x16x32_bf16 v[140:143], v[128:131], v[124:127], v[140:143]\n"
"s_setprio 0\n"
"s_add_u32 m0, s55, 36864\n"
"s_nop 0\n"
"global_load_lds_dwordx4 v22, s[60:61]\n"
"s_add_u32 m0, s55, 8192\n"
"s_nop 0\n"
"global_load_lds_dwordx4 v18, s[56:57]\n"
"s_add_u32 m0, s55, 28672\n"
"s_nop 0\n"
"global_load_lds_dwordx4 v23, s[58:59]\n"
"s_add_u32 m0, s55, 12288\n"
"v_cvt_pk_bf16_f32 v133, v132, v132\n"
"global_load_lds_dwordx4 v19, s[56:57]\n"
"v_lshlrev_b32_e64 v133, 16, v133\n"
"s_add_u32 m0, s55, 40960\n"
"v_sub_f32_e64 v132, v132, v133\n"
"global_load_lds_dwordx4 v23, s[60:61]\n"
"v_cvt_pk_bf16_f32 v100, v133, v132\n"
"s_add_u32 m0, s55, 16384\n"
"v_cvt_pk_bf16_f32 v137, v136, v136\n"
"global_load_lds_dwordx4 v20, s[56:57]\n"
"v_lshlrev_b32_e64 v137, 16, v137\n"
"s_add_u32 m0, s55, 32768\n"
"v_sub_f32_e64 v136, v136, v137\n"
"global_load_lds_dwordx4 v24, s[58:59]\n"
"v_cvt_pk_bf16_f32 v101, v137, v136\n"
"s_add_u32 m0, s55, 20480\n"
"v_cvt_pk_bf16_f32 v141, v140, v140\n"
"global_load_lds_dwordx4 v21, s[56:57]\n"
"v_lshlrev_b32_e64 v141, 16, v141\n"
"s_add_u32 m0, s55, 45056\n"
"v_sub_f32_e64 v140, v140, v141\n"
"global_load_lds_dwordx4 v24, s[60:61]\n"
"v_cvt_pk_bf16_f32 v102, v141, v140\n"
"s_cmp_eq_u32 s47, 15\n"
"s_cbranch_scc1 .Lfb_H_CN\n"
"s_mov_b64 exec, 0xffff\n"
"global_store_dwordx4 v96, v[100:103], s[18:19] sc1\n"
"s_mov_b64 exec, -1\n"
".Lfb_H_CN:\n"
"s_waitcnt vmcnt(12)\n"
"s_barrier\n"
"s_and_b32 s34, s46, 7\n"
"s_lshr_b32 s46, s46, 4\n"
"s_lshl_b32 s35, s34, 7\n"
"s_mul_i32 s34, s34, s54\n"
"s_add_u32 s56, s48, s35\n"
"s_addc_u32 s57, s49, 0\n"
"s_add_u32 s58, s50, s34\n"
"s_addc_u32 s59, s51, 0\n"
"s_add_u32 s60, s52, s34\n"
"s_addc_u32 s61, s53, 0\n"
"s_add_u32 m0, s55, 49152\n"
"s_nop 0\n"
"global_load_lds_dwordx4 v16, s[56:57]\n"
"s_and_b32 s34, s26, 3\n"
"s_and_b32 s29, s2, 7\n"
"s_lshl2_add_u32 s29, s29, s34\n"
"s_lshr_b32 s30, s26, 2\n"
"s_add_u32 m0, s55, 73728\n"
"s_nop 0\n"
"s_nop 0\n"
"global_load_lds_dwordx4 v22, s[58:59]\n"
"s_sub_u32 s34, 0x1ff, s14\n"
"s_bfe_u32 s34, s34, 0x30007\n"
"s_xor_b32 s30, s30, s34\n"
"s_xor_b32 s30, s30, 0\n"
"s_add_u32 m0, s55, 53248\n"
"s_nop 0\n"
"global_load_lds_dwordx4 v17, s[56:57]\n"
"s_mov_b32 s31, 1000000\n"
"s_lshl_b32 s38, s30, 7\n"
"s_mul_i32 s39, s20, 344064\n"
"s_add_u32 s39, s39, s38\n"
"s_add_u32 m0, s55, 86016\n"
"s_nop 0\n"
"global_load_lds_dwordx4 v22, s[60:61]\n"
"s_add_u32 s66, s14, s39\n"
"s_addc_u32 s67, s15, 0\n"
"s_mul_i32 s39, s20, 393216\n"
"s_add_u32 s39, s39, s38\n"
"s_add_u32 m0, s55, 57344\n"
"s_nop 0\n"
"global_load_lds_dwordx4 v18, s[56:57]\n"
"s_add_u32 s39, s39, 32768\n"
"s_add_u32 s68, s14, s39\n"
"s_addc_u32 s69, s15, 0\n"
"s_mov_b32 s70, 196608\n"
"s_add_u32 m0, s55, 77824\n"
"s_nop 0\n"
"s_nop 0\n"
"global_load_lds_dwordx4 v23, s[58:59]\n"
"s_mov_b32 s71, 245760\n"
"s_mov_b32 s72, 294912\n"
"s_mov_b32 s73, 344064\n"
"s_cmp_eq_u32 s20, 3\n"
"s_cselect_b32 s70, 163840, s70\n"
"s_cselect_b32 s71, 180224, s71\n"
"s_cselect_b32 s72, 0, s72\n"
"s_cselect_b32 s73, 0, s73\n"
"s_add_u32 m0, s55, 61440\n"
"s_nop 0\n"
"global_load_lds_dwordx4 v19, s[56:57]\n"
"s_mul_i32 s74, s20, 12288\n"
"s_add_u32 m0, s55, 90112\n"
"s_nop 0\n"
"global_load_lds_dwordx4 v23, s[60:61]\n"
"s_add_u32 s74, s74, 98304\n"
"v_lshrrev_b32 v9, 4, v0\n"
"v_and_b32 v10, 7, v0\n"
"v_lshlrev_b32 v13, 1, v9\n"
"s_add_u32 m0, s55, 65536\n"
"s_nop 0\n"
"global_load_lds_dwordx4 v20, s[56:57]\n"
"v_xor_b32 v13, v13, v10\n"
"v_lshlrev_b32 v13, 4, v13\n"
"v_lshrrev_b32 v8, 3, v0\n"
"v_lshl_add_u32 v32, v8, 10, v13\n"
"s_add_u32 m0, s55, 81920\n"
"s_nop 0\n"
"s_nop 0\n"
"global_load_lds_dwordx4 v24, s[58:59]\n"
"v_add_u32 v33, 0x2000, v32\n"
"v_add_u32 v34, 0x4000, v32\n"
"v_add_u32 v35, 0x6000, v32\n"
"v_lshrrev_b32_e64 v8, 5, v0\n"
"s_add_u32 m0, s55, 69632\n"
"s_nop 0\n"
"global_load_lds_dwordx4 v21, s[56:57]\n"
"v_lshl_add_u32 v36, v8, 12, v32\n"
"v_add_u32 v37, 0x1000, v36\n"
"v_lshrrev_b32 v8, 2, v0\n"
"v_bfe_u32 v9, v0, 3, 2\n"
"v_and_b32_e64 v10, 3, v0\n"
"s_add_u32 m0, s55, 94208\n"
"s_nop 0\n"
"global_load_lds_dwordx4 v24, s[60:61]\n"
"s_waitcnt vmcnt(12)\n"
"s_barrier\n"
"s_and_b32 s34, s46, 7\n"
"s_lshr_b32 s46, s46, 4\n"
"s_lshl_b32 s35, s34, 7\n"
"s_mul_i32 s34, s34, s54\n"
"s_add_u32 s56, s48, s35\n"
"s_addc_u32 s57, s49, 0\n"
"s_add_u32 s58, s50, s34\n"
"s_addc_u32 s59, s51, 0\n"
"s_add_u32 s60, s52, s34\n"
"s_addc_u32 s61, s53, 0\n"
"s_add_u32 m0, s55, 98304\n"
"s_nop 0\n"
"global_load_lds_dwordx4 v16, s[56:57]\n"
"v_lshlrev_b32_e64 v8, 7, v8\n"
"v_lshl_add_u32 v8, v9, 5, v8\n"
"v_lshl_add_u32 v8, v10, 3, v8\n"
"v_add_u32_e64 v1, s74, v8\n"
"s_add_u32 m0, s55, 122880\n"
"s_nop 0\n"
"global_load_lds_dwordx4 v22, s[58:59]\n"
"v_xor_b32 v2, 64, v1\n"
"v_xor_b32 v38, 32, v1\n"
"v_xor_b32 v39, 32, v2\n"
"s_lshl2_add_u32 s38, s20, 161824\n"
"s_add_u32 m0, s55, 102400\n"
"s_nop 0\n"
"global_load_lds_dwordx4 v17, s[56:57]\n"
"v_mov_b32 v3, s38\n"
"v_lshlrev_b32_e64 v10, 2, v10\n"
"v_add_u32 v4, 161840, v10\n"
"s_lshr_b32 s38, s30, 2\n"
"s_add_u32 m0, s55, 135168\n"
"s_nop 0\n"
"global_load_lds_dwordx4 v22, s[60:61]\n"
"s_lshl3_add_u32 s38, s29, s38\n"
"s_lshl2_add_u32 s38, s38, s20\n"
"s_lshl_b32 s38, s38, 8\n"
"s_add_u32 s75, s38, 10485760\n"
"s_mul_i32 s38, s29, 43\n"
"s_add_u32 m0, s55, 106496\n"
"s_nop 0\n"
"global_load_lds_dwordx4 v18, s[56:57]\n"
"s_lshr_b32 s38, s38, 7\n"
"s_mul_i32 s38, s38, 3\n"
"s_sub_u32 s38, s29, s38\n"
"s_lshl_b32 s38, s38, 3\n"
"s_add_u32 m0, s55, 126976\n"
"s_nop 0\n"
"global_load_lds_dwordx4 v23, s[58:59]\n"
"s_lshl_b32 s38, 0xffff, s38\n"
"s_bfe_u32 s38, s38, 0x100008\n"
"s_and_b32 s42, s38, 0x7f7f\n"
"s_and_b32 s44, s38, 0x3f3f\n"
"s_add_u32 m0, s55, 110592\n"
"s_nop 0\n"
"global_load_lds_dwordx4 v19, s[56:57]\n"
"s_mov_b32 s43, 0\n"
"s_cmp_lt_u32 s20, 3\n"
"s_cselect_b32 s44, s38, s44\n"
"s_mov_b32 s45, 0\n"
"s_add_u32 m0, s55, 139264\n"
"s_nop 0\n"
"global_load_lds_dwordx4 v23, s[60:61]\n"
"s_mul_i32 s38, s20, 15\n"
"s_cmp_eq_u32 s20, 3\n"
"s_cselect_b32 s38, 0, s38\n"
"s_cselect_b32 s39, 16, 43\n"
"s_cselect_b64 s[0:1], s[8:9], s[14:15]\n"
"s_add_u32 m0, s55, 114688\n"
"s_nop 0\n"
"global_load_lds_dwordx4 v20, s[56:57]\n"
"s_mul_i32 s40, s39, s26\n"
"v_add_u32 v60, s38, v0\n"
"v_min_u32 v60, 42, v60\n"
"v_add_u32 v60, s40, v60\n"
"s_add_u32 m0, s55, 131072\n"
"s_nop 0\n"
"s_nop 0\n"
"global_load_lds_dwordx4 v24, s[58:59]\n"
"s_sub_u32 s40, 0x1ff, s0\n"
"s_and_b32 s40, s40, 0x380\n"
"v_lshl_add_u32 v60, v60, 10, s40\n"
"s_mul_i32 s39, s39, s29\n"
"s_add_u32 m0, s55, 118784\n"
"s_nop 0\n"
"global_load_lds_dwordx4 v21, s[56:57]\n"
"v_add_u32 v61, s38, v0\n"
"v_min_u32 v61, 42, v61\n"
"v_add_u32 v61, s39, v61\n"
"s_lshl_b32 s38, s30, 7\n"
"v_lshl_add_u32 v61, v61, 10, s38\n"
"s_add_u32 m0, s55, 143360\n"
"s_nop 0\n"
"global_load_lds_dwordx4 v24, s[60:61]\n"
"s_mov_b64 exec, 0xffff\n"
"global_load_dword v62, v61, s[0:1]\n"
"s_mov_b64 exec, -1\n"
"s_waitcnt vmcnt(13)\n"
"s_barrier\n"
"s_and_b32 s34, s46, 7\n"
"s_lshr_b32 s46, s46, 4\n"
"s_lshl_b32 s35, s34, 7\n"
"s_mul_i32 s34, s34, s54\n"
"s_add_u32 s56, s48, s35\n"
"s_addc_u32 s57, s49, 0\n"
"s_add_u32 s58, s50, s34\n"
"s_addc_u32 s59, s51, 0\n"
"s_add_u32 s60, s52, s34\n"
"s_addc_u32 s61, s53, 0\n"
"s_add_u32 m0, s55, 0\n"
"s_nop 0\n"
"global_load_lds_dwordx4 v16, s[56:57]\n"
"s_add_u32 m0, s55, 24576\n"
"s_nop 0\n"
"global_load_lds_dwordx4 v22, s[58:59]\n"
"s_add_u32 m0, s55, 4096\n"
"s_nop 0\n"
"global_load_lds_dwordx4 v17, s[56:57]\n"
"s_add_u32 m0, s55, 36864\n"
"s_nop 0\n"
"global_load_lds_dwordx4 v22, s[60:61]\n"
"s_add_u32 m0, s55, 8192\n"
"s_nop 0\n"
"global_load_lds_dwordx4 v18, s[56:57]\n"
"s_add_u32 m0, s55, 28672\n"
"s_nop 0\n"
"global_load_lds_dwordx4 v23, s[58:59]\n"
"s_add_u32 m0, s55, 12288\n"
"s_nop 0\n"
"global_load_lds_dwordx4 v19, s[56:57]\n"
"s_add_u32 m0, s55, 40960\n"
"s_nop 0\n"
"global_load_lds_dwordx4 v23, s[60:61]\n"
"s_add_u32 m0, s55, 16384\n"
"s_nop 0\n"
"global_load_lds_dwordx4 v20, s[56:57]\n"
"s_add_u32 m0, s55, 32768\n"
"s_nop 0\n"
"global_load_lds_dwordx4 v24, s[58:59]\n"
"s_add_u32 m0, s55, 20480\n"
"s_nop 0\n"
"global_load_lds_dwordx4 v21, s[56:57]\n"
"s_add_u32 m0, s55, 45056\n"
"s_nop 0\n"
"s_nop 0\n"
"global_load_lds_dwordx4 v24, s[60:61]\n"
"s_mov_b64 exec, 0xffff\n"
"global_load_dword v63, v60, s[0:1]\n"
"s_mov_b64 exec, -1\n"
"s_waitcnt vmcnt(13)\n"
"s_barrier\n"
"s_lshl_b32 s34, s20, 11\n"
"s_mul_i32 s35, s20, 2056\n"
"s_add_u32 m0, s35, 147456\n"
"s_nop 0\n"
"global_load_lds_dwordx4 v80, s[64:65] sc1\n"
"s_add_u32 m0, s35, 148480\n"
"s_nop 0\n"
"global_load_lds_dwordx4 v81, s[64:65] sc1\n"
"s_cmp_eq_u32 s20, 3\n"
"s_cbranch_scc1 .Lfb_TL_2\n"
"s_add_u32 m0, s34, 155680\n"
"s_nop 0\n"
"global_load_lds_dwordx4 v82, s[76:77] sc1\n"
"s_add_u32 m0, s34, 156704\n"
"s_nop 0\n"
"global_load_lds_dwordx4 v83, s[76:77] sc1\n"
".Lfb_TL_2:\n"
"s_and_b32 s34, s46, 7\n"
"s_lshr_b32 s46, s46, 4\n"
"s_lshl_b32 s35, s34, 7\n"
"s_mul_i32 s34, s34, s54\n"
"s_add_u32 s56, s48, s35\n"
"s_addc_u32 s57, s49, 0\n"
"s_add_u32 s58, s50, s34\n"
"s_addc_u32 s59, s51, 0\n"
"s_add_u32 s60, s52, s34\n"
"s_addc_u32 s61, s53, 0\n"
"s_add_u32 m0, s55, 49152\n"
"s_nop 0\n"
"global_load_lds_dwordx4 v16, s[56:57]\n"
"s_add_u32 m0, s55, 73728\n"
"s_nop 0\n"
"global_load_lds_dwordx4 v22, s[58:59]\n"
"s_add_u32 m0, s55, 53248\n"
"s_nop 0\n"
"global_load_lds_dwordx4 v17, s[56:57]\n"
"s_add_u32 m0, s55, 86016\n"
"s_nop 0\n"
"global_load_lds_dwordx4 v22, s[60:61]\n"
"s_add_u32 m0, s55, 57344\n"
"s_nop 0\n"
"global_load_lds_dwordx4 v18, s[56:57]\n"
"s_add_u32 m0, s55, 77824\n"
"s_nop 0\n"
"global_load_lds_dwordx4 v23, s[58:59]\n"
"s_add_u32 m0, s55, 61440\n"
"s_nop 0\n"
"global_load_lds_dwordx4 v19, s[56:57]\n"
"s_add_u32 m0, s55, 90112\n"
"s_nop 0\n"
"global_load_lds_dwordx4 v23, s[60:61]\n"
"s_add_u32 m0, s55, 65536\n"
"s_nop 0\n"
"global_load_lds_dwordx4 v20, s[56:57]\n"
"s_add_u32 m0, s55, 81920\n"
"s_nop 0\n"
"global_load_lds_dwordx4 v24, s[58:59]\n"
"s_add_u32 m0, s55, 69632\n"
"s_nop 0\n"
"global_load_lds_dwordx4 v21, s[56:57]\n"
"s_add_u32 m0, s55, 94208\n"
"s_nop 0\n"
"global_load_lds_dwordx4 v24, s[60:61]\n"
"s_waitcnt vmcnt(12)\n"
"s_barrier\n"
"s_add_u32 s58, s66, 0\n"
"s_addc_u32 s59, s67, 0\n"
"s_add_u32 m0, s74, 0\n"
"s_nop 0\n"
"global_load_lds_dwordx4 v32, s[58:59]\n"
"s_add_u32 m0, s74, 1024\n"
"s_nop 0\n"
"global_load_lds_dwordx4 v33, s[58:59]\n"
"s_add_u32 m0, s74, 2048\n"
"s_nop 0\n"
"global_load_lds_dwordx4 v34, s[58:59]\n"
"s_add_u32 m0, s74, 3072\n"
"s_nop 0\n"
"global_load_lds_dwordx4 v35, s[58:59]\n"
"s_add_u32 s58, s66, 49152\n"
"s_addc_u32 s59, s67, 0\n"
"s_add_u32 m0, s74, 4096\n"
"s_nop 0\n"
"global_load_lds_dwordx4 v32, s[58:59]\n"
"s_add_u32 m0, s74, 5120\n"
"s_nop 0\n"
"global_load_lds_dwordx4 v33, s[58:59]\n"
"s_add_u32 m0, s74, 6144\n"
"s_nop 0\n"
"global_load_lds_dwordx4 v34, s[58:59]\n"
"s_add_u32 m0, s74, 7168\n"
"s_nop 0\n"
"global_load_lds_dwordx4 v35, s[58:59]\n"
"s_add_u32 s58, s66, 98304\n"
"s_addc_u32 s59, s67, 0\n"
"s_add_u32 m0, s74, 8192\n"
"s_nop 0\n"
"global_load_lds_dwordx4 v32, s[58:59]\n"
"s_add_u32 m0, s74, 9216\n"
"s_nop 0\n"
"global_load_lds_dwordx4 v33, s[58:59]\n"
"s_waitcnt vmcnt(10)\n"
".Lfb_H_GC:\n"
"ds_read_b32 v84, v88\n"
"ds_read_b32 v85, v88 offset:1024\n"
"ds_read_b32 v86, v89\n"
"ds_read_b32 v87, v89 offset:1024\n"
"s_waitcnt lgkmcnt(0)\n"
"s_nop 0\n"
"v_min3_u32 v90, v84, v85, v86\n"
"v_max3_u32 v91, v84, v85, v86\n"
"v_min3_u32 v90, v90, v87, s32\n"
"v_max3_u32 v91, v91, v87, s32\n"
"v_cmp_ne_u32 vcc, v90, v91\n"
"s_nop 0\n"
"s_cbranch_vccnz .Lfb_H_GR\n"
".Lfb_H_GO:\n"
"s_waitcnt vmcnt(10)\n"
"s_barrier\n"
".Lfb_H_P2:\n"
"s_cmp_ge_u32 s22, 2\n"
"s_cselect_b64 vcc, -1, 0\n"
"s_lshl_b32 s79, s20, 12\n"
"s_lshl_b32 s34, s30, 7\n"
"s_lshl3_add_u32 s34, s79, s34\n"
"s_add_u32 s56, s8, s34\n"
"s_addc_u32 s57, s9, 0\n"
"s_add_u32 m0, s74, 10240\n"
"s_nop 0\n"
"global_load_lds_dwordx4 v34, s[58:59]\n"
"s_add_u32 m0, s74, 11264\n"
"s_nop 0\n"
"global_load_lds_dwordx4 v35, s[58:59]\n"
"s_waitcnt vmcnt(10)\n"
"s_nop 0\n"
"ds_read_b64_tr_b16 v[128:129], v1 offset:0\n"
"ds_read_b64_tr_b16 v[132:133], v38 offset:0\n"
"ds_read_b64_tr_b16 v[136:137], v2 offset:0\n"
"ds_read_b64_tr_b16 v[140:141], v39 offset:0\n"
"s_add_u32 s50, s56, 0\n"
"s_addc_u32 s51, s57, 0\n"
"s_add_u32 m0, s79, 0\n"
"s_nop 0\n"
"global_load_lds_dwordx4 v36, s[50:51]\n"
"s_waitcnt vmcnt(9)\n"
"s_nop 0\n"
"ds_read_b64_tr_b16 v[130:131], v1 offset:2048\n"
"ds_read_b64_tr_b16 v[134:135], v38 offset:2048\n"
"ds_read_b64_tr_b16 v[138:139], v2 offset:2048\n"
"ds_read_b64_tr_b16 v[142:143], v39 offset:2048\n"
"s_add_u32 s58, s66, 147456\n"
"s_addc_u32 s59, s67, 0\n"
"s_add_u32 m0, s74, 0\n"
"s_waitcnt lgkmcnt(4)\n"
"global_load_lds_dwordx4 v32, s[58:59]\n"
"s_add_u32 m0, s74, 1024\n"
"s_nop 0\n"
"global_load_lds_dwordx4 v33, s[58:59]\n"
"s_add_u32 s52, s56, 16384\n"
"s_addc_u32 s53, s57, 0\n"
"s_add_u32 m0, s79, 1024\n"
"s_nop 0\n"
"global_load_lds_dwordx4 v36, s[52:53]\n"
"s_waitcnt vmcnt(10)\n"
"s_nop 0\n"
"ds_read_b64_tr_b16 v[144:145], v1 offset:4096\n"
"ds_read_b64_tr_b16 v[148:149], v38 offset:4096\n"
"ds_read_b64_tr_b16 v[152:153], v2 offset:4096\n"
"ds_read_b64_tr_b16 v[156:157], v39 offset:4096\n"
"s_add_u32 m0, s74, 2048\n"
"s_waitcnt lgkmcnt(4)\n"
"global_load_lds_dwordx4 v34, s[58:59]\n"
"s_add_u32 m0, s74, 3072\n"
"s_nop 0\n"
"global_load_lds_dwordx4 v35, s[58:59]\n"
"s_add_u32 m0, s79, 2048\n"
"s_nop 0\n"
"global_load_lds_dwordx4 v37, s[50:51]\n"
"s_waitcnt vmcnt(11)\n"
"ds_read_b64_tr_b16 v[146:147], v1 offset:6144\n"
"ds_read_b64_tr_b16 v[150:151], v38 offset:6144\n"
"ds_read_b64_tr_b16 v[154:155], v2 offset:6144\n"
"ds_read_b64_tr_b16 v[158:159], v39 offset:6144\n"
"s_add_u32 s58, s66, 196608\n"
"s_addc_u32 s59, s67, 0\n"
"s_add_u32 m0, s74, 4096\n"
"s_waitcnt lgkmcnt(4)\n"
"global_load_lds_dwordx4 v32, s[58:59]\n"
"s_add_u32 m0, s74, 5120\n"
"s_nop 0\n"
"global_load_lds_dwordx4 v33, s[58:59]\n"
"s_add_u32 m0, s79, 3072\n"
"s_nop 0\n"
"global_load_lds_dwordx4 v37, s[52:53]\n"
"s_waitcnt vmcnt(11)\n"
"s_nop 0\n"
"ds_read_b64_tr_b16 v[160:161], v1 offset:8192\n"
"ds_read_b64_tr_b16 v[164:165], v38 offset:8192\n"
"ds_read_b64_tr_b16 v[168:169], v2 offset:8192\n"
"ds_read_b64_tr_b16 v[172:173], v39 offset:8192\n"
"s_add_u32 m0, s74, 6144\n"
"s_waitcnt lgkmcnt(4)\n"
"global_load_lds_dwordx4 v34, s[58:59]\n"
"s_add_u32 m0, s74, 7168\n"
"s_nop 0\n"
"global_load_lds_dwordx4 v35, s[58:59]\n"
"s_add_u32 s50, s56, 131072\n"
"s_addc_u32 s51, s57, 0\n"
"s_add_u32 m0, s79, 16384\n"
"s_nop 0\n"
"global_load_lds_dwordx4 v36, s[50:51]\n"
"s_waitcnt vmcnt(11)\n"
"s_nop 0\n"
"ds_read_b64_tr_b16 v[162:163], v1 offset:10240\n"
"ds_read_b64_tr_b16 v[166:167], v38 offset:10240\n"
"ds_read_b64_tr_b16 v[170:171], v2 offset:10240\n"
"ds_read_b64_tr_b16 v[174:175], v39 offset:10240\n"
"s_add_u32 s58, s66, 245760\n"
"s_addc_u32 s59, s67, 0\n"
"s_add_u32 m0, s74, 8192\n"
"s_waitcnt lgkmcnt(4)\n"
"global_load_lds_dwordx4 v32, s[58:59]\n"
"s_add_u32 m0, s74, 9216\n"
"s_nop 0\n"
"global_load_lds_dwordx4 v33, s[58:59]\n"
"s_add_u32 s52, s56, 147456\n"
"s_addc_u32 s53, s57, 0\n"
"s_add_u32 m0, s79, 17408\n"
"s_nop 0\n"
"global_load_lds_dwordx4 v36, s[52:53]\n"
"s_waitcnt vmcnt(11)\n"
"ds_read_b64_tr_b16 v[176:177], v1 offset:0\n"
"ds_read_b64_tr_b16 v[180:181], v38 offset:0\n"
"ds_read_b64_tr_b16 v[184:185], v2 offset:0\n"
"ds_read_b64_tr_b16 v[188:189], v39 offset:0\n"
"s_add_u32 m0, s74, 10240\n"
"s_waitcnt lgkmcnt(4)\n"
"global_load_lds_dwordx4 v34, s[58:59]\n"
"s_add_u32 m0, s74, 11264\n"
"s_nop 0\n"
"global_load_lds_dwordx4 v35, s[58:59]\n"
"s_add_u32 m0, s79, 18432\n"
"s_nop 0\n"
"global_load_lds_dwordx4 v37, s[50:51]\n"
"s_waitcnt vmcnt(3)\n"
"ds_read_b64_tr_b16 v[178:179], v1 offset:2048\n"
"ds_read_b64_tr_b16 v[182:183], v38 offset:2048\n"
"ds_read_b64_tr_b16 v[186:187], v2 offset:2048\n"
"ds_read_b64_tr_b16 v[190:191], v39 offset:2048\n"
"s_add_u32 s58, s66, 294912\n"
"s_addc_u32 s59, s67, 0\n"
"s_add_u32 m0, s74, 0\n"
"s_waitcnt lgkmcnt(4)\n"
"global_load_lds_dwordx4 v32, s[58:59]\n"
"s_add_u32 m0, s74, 1024\n"
"s_nop 0\n"
"global_load_lds_dwordx4 v33, s[58:59]\n"
"s_add_u32 m0, s79, 19456\n"
"s_nop 0\n"
"global_load_lds_dwordx4 v37, s[52:53]\n"
"s_waitcnt vmcnt(3)\n"
"ds_read_b64_tr_b16 v[192:193], v1 offset:4096\n"
"ds_read_b64_tr_b16 v[196:197], v38 offset:4096\n"
"ds_read_b64_tr_b16 v[200:201], v2 offset:4096\n"
"ds_read_b64_tr_b16 v[204:205], v39 offset:4096\n"
"s_add_u32 m0, s74, 2048\n"
"s_waitcnt lgkmcnt(4)\n"
"global_load_lds_dwordx4 v34, s[58:59]\n"
"s_add_u32 m0, s74, 3072\n"
"s_nop 0\n"
"global_load_lds_dwordx4 v35, s[58:59]\n"
"s_add_u32 s50, s56, 262144\n"
"s_addc_u32 s51, s57, 0\n"
"s_add_u32 m0, s79, 32768\n"
"s_nop 0\n"
"global_load_lds_dwordx4 v36, s[50:51]\n"
"s_waitcnt vmcnt(3)\n"
"s_nop 0\n"
"ds_read_b64_tr_b16 v[194:195], v1 offset:6144\n"
"ds_read_b64_tr_b16 v[198:199], v38 offset:6144\n"
"ds_read_b64_tr_b16 v[202:203], v2 offset:6144\n"
"ds_read_b64_tr_b16 v[206:207], v39 offset:6144\n"
"s_add_u32 s58, s68, 0\n"
"s_addc_u32 s59, s69, 0\n"
"s_add_u32 m0, s74, 4096\n"
"s_waitcnt lgkmcnt(4)\n"
"global_load_lds_dwordx4 v36, s[58:59]\n"
"s_add_u32 s60, s68, 49152\n"
"s_addc_u32 s61, s69, 0\n"
"s_add_u32 m0, s74, 5120\n"
"s_nop 0\n"
"global_load_lds_dwordx4 v36, s[60:61]\n"
"s_add_u32 s52, s56, 278528\n"
"s_addc_u32 s53, s57, 0\n"
"s_add_u32 m0, s79, 33792\n"
"s_nop 0\n"
"global_load_lds_dwordx4 v36, s[52:53]\n"
"s_waitcnt vmcnt(3)\n"
"ds_read_b64_tr_b16 v[208:209], v1 offset:8192\n"
"ds_read_b64_tr_b16 v[212:213], v38 offset:8192\n"
"ds_read_b64_tr_b16 v[216:217], v2 offset:8192\n"
"ds_read_b64_tr_b16 v[220:221], v39 offset:8192\n"
"s_add_u32 m0, s74, 6144\n"
"s_waitcnt lgkmcnt(4)\n"
"global_load_lds_dwordx4 v37, s[58:59]\n"
"s_add_u32 m0, s74, 7168\n"
"s_nop 0\n"
"global_load_lds_dwordx4 v37, s[60:61]\n"
"s_add_u32 m0, s79, 34816\n"
"s_nop 0\n"
"global_load_lds_dwordx4 v37, s[50:51]\n"
"s_waitcnt vmcnt(3)\n"
"ds_read_b64_tr_b16 v[210:211], v1 offset:10240\n"
"ds_read_b64_tr_b16 v[214:215], v38 offset:10240\n"
"ds_read_b64_tr_b16 v[218:219], v2 offset:10240\n"
"ds_read_b64_tr_b16 v[222:223], v39 offset:10240\n"
"s_add_u32 s58, s68, 98304\n"
"s_addc_u32 s59, s69, 0\n"
"s_add_u32 m0, s74, 8192\n"
"s_waitcnt lgkmcnt(4)\n"
"global_load_lds_dwordx4 v36, s[58:59]\n"
"s_add_u32 s60, s68, 147456\n"
"s_addc_u32 s61, s69, 0\n"
"s_add_u32 m0, s74, 9216\n"
"s_nop 0\n"
"global_load_lds_dwordx4 v36, s[60:61]\n"
"s_add_u32 m0, s79, 35840\n"
"s_nop 0\n"
"global_load_lds_dwordx4 v37, s[52:53]\n"
"s_waitcnt vmcnt(4)\n"
"ds_read_b64_tr_b16 v[224:225], v1 offset:0\n"
"ds_read_b64_tr_b16 v[228:229], v38 offset:0\n"
"ds_read_b64_tr_b16 v[232:233], v2 offset:0\n"
"ds_read_b64_tr_b16 v[236:237], v39 offset:0\n"
"s_add_u32 m0, s74, 10240\n"
"s_waitcnt lgkmcnt(4)\n"
"global_load_lds_dwordx4 v37, s[58:59]\n"
"s_add_u32 m0, s74, 11264\n"
"s_nop 0\n"
"global_load_lds_dwordx4 v37, s[60:61]\n"
"s_waitcnt vmcnt(7)\n"
"s_nop 0\n"
"ds_read_b64_tr_b16 v[226:227], v1 offset:2048\n"
"ds_read_b64_tr_b16 v[230:231], v38 offset:2048\n"
"ds_read_b64_tr_b16 v[234:235], v2 offset:2048\n"
"ds_read_b64_tr_b16 v[238:239], v39 offset:2048\n"
"s_add_u32 s58, s68, s70\n"
"s_addc_u32 s59, s69, 0\n"
"s_add_u32 m0, s74, 0\n"
"s_waitcnt lgkmcnt(4)\n"
"global_load_lds_dwordx4 v36, s[58:59]\n"
"s_add_u32 s60, s68, s71\n"
"s_addc_u32 s61, s69, 0\n"
"s_add_u32 m0, s74, 1024\n"
"s_nop 0\n"
"global_load_lds_dwordx4 v36, s[60:61]\n"
"s_waitcnt vmcnt(7)\n"
"ds_read_b64_tr_b16 v[240:241], v1 offset:4096\n"
"ds_read_b64_tr_b16 v[244:245], v38 offset:4096\n"
"ds_read_b64_tr_b16 v[248:249], v2 offset:4096\n"
"ds_read_b64_tr_b16 v[252:253], v39 offset:4096\n"
"s_add_u32 m0, s74, 2048\n"
"s_waitcnt lgkmcnt(4)\n"
"global_load_lds_dwordx4 v37, s[58:59]\n"
"s_add_u32 m0, s74, 3072\n"
"s_nop 0\n"
"global_load_lds_dwordx4 v37, s[60:61]\n"
"s_waitcnt vmcnt(7)\n"
"s_nop 0\n"
"ds_read_b64_tr_b16 v[242:243], v1 offset:6144\n"
"ds_read_b64_tr_b16 v[246:247], v38 offset:6144\n"
"ds_read_b64_tr_b16 v[250:251], v2 offset:6144\n"
"ds_read_b64_tr_b16 v[254:255], v39 offset:6144\n"
"s_add_u32 s58, s68, s72\n"
"s_addc_u32 s59, s69, 0\n"
"s_add_u32 m0, s74, 4096\n"
"s_waitcnt lgkmcnt(4)\n"
"global_load_lds_dwordx4 v36, s[58:59]\n"
"s_add_u32 s60, s68, s73\n"
"s_addc_u32 s61, s69, 0\n"
"s_add_u32 m0, s74, 5120\n"
"s_nop 0\n"
"global_load_lds_dwordx4 v36, s[60:61]\n"
"s_add_u32 m0, s74, 6144\n"
"s_waitcnt lgkmcnt(0)\n"
"global_load_lds_dwordx4 v37, s[58:59]\n"
"s_add_u32 m0, s74, 7168\n"
"s_nop 0\n"
"global_load_lds_dwordx4 v37, s[60:61]\n"
".Lfb_LP_3:\n"
"ds_read_b32 v5, v4 offset:0\n"
"s_waitcnt lgkmcnt(0)\n"
"v_cmp_gt_u32 vcc, 1, v5\n"
"s_nop 0\n"
"s_cbranch_vccz .Lfb_LD_4\n"
"s_sleep 4\n"
"s_sub_u32 s31, s31, 1\n"
"s_cbranch_scc0 .Lfb_LP_3\n"
"s_mov_b32 s31, 0\n"
".Lfb_LD_4:\n"
"s_add_u32 s50, s56, 393216\n"
"s_addc_u32 s51, s57, 0\n"
"s_add_u32 m0, s79, 49152\n"
"s_nop 0\n"
"global_load_lds_dwordx4 v36, s[50:51]\n"
"s_add_u32 s52, s56, 409600\n"
"s_addc_u32 s53, s57, 0\n"
"s_add_u32 m0, s79, 50176\n"
"s_nop 0\n"
"global_load_lds_dwordx4 v36, s[52:53]\n"
"s_add_u32 m0, s79, 51200\n"
"s_nop 0\n"
"global_load_lds_dwordx4 v37, s[50:51]\n"
"s_add_u32 m0, s79, 52224\n"
"s_nop 0\n"
"global_load_lds_dwordx4 v37, s[52:53]\n"
".Lfb_H_NW:\n"
"s_add_u32 s60, s18, s75\n"
"s_addc_u32 s61, s19, 0\n"
"s_mul_i32 s34, s29, 112\n"
"s_mul_i32 s35, s20, 7\n"
"s_add_u32 s34, s34, s35\n"
"s_lshl_b32 s34, s34, 10\n"
"s_nop 0\n"
"s_add_u32 s34, s34, 1051648\n"
"s_add_u32 s66, s18, s34\n"
"s_addc_u32 s67, s19, 0\n"
"s_lshl3_add_u32 s34, s29, s20\n"
"s_lshl_b32 s34, s34, 13\n"
"s_add_u32 s34, s34, 5246976\n"
"s_add_u32 s68, s18, s34\n"
"s_addc_u32 s69, s19, 0\n"
"v_lshlrev_b32 v5, 4, v0\n"
"v_and_b32 v6, 15, v0\n"
"v_lshlrev_b32 v6, 4, v6\n"
"v_lshrrev_b32 v7, 4, v0\n"
"v_lshl_add_u32 v6, v7, 9, v6\n"
"s_add_u32 s34, s20, 1\n"
"s_and_b32 s34, s34, 3\n"
"s_mul_i32 s35, s34, 28672\n"
"v_add_u32_e64 v8, s35, v5\n"
"s_mul_i32 s34, s34, 0x4100\n"
"s_and_b32 s34, s34, 0x8100\n"
"v_add_u32_e64 v12, s34, v6\n"
"s_add_u32 s34, s20, 2\n"
"s_and_b32 s34, s34, 3\n"
"s_mul_i32 s35, s34, 28672\n"
"v_add_u32_e64 v9, s35, v5\n"
"s_mul_i32 s34, s34, 0x4100\n"
"s_and_b32 s34, s34, 0x8100\n"
"v_add_u32_e64 v13, s34, v6\n"
"s_add_u32 s34, s20, 3\n"
"s_and_b32 s34, s34, 3\n"
"s_mul_i32 s35, s34, 28672\n"
"v_add_u32_e64 v10, s35, v5\n"
"s_mul_i32 s34, s34, 0x4100\n"
"s_and_b32 s34, s34, 0x8100\n"
"v_add_u32_e64 v14, s34, v6\n"
"s_add_u32 s34, s20, 4\n"
"s_and_b32 s34, s34, 3\n"
"s_mul_i32 s35, s34, 28672\n"
"v_add_u32_e64 v11, s35, v5\n"
"s_mul_i32 s34, s34, 0x4100\n"
"s_and_b32 s34, s34, 0x8100\n"
"v_add_u32 v15, s34, v6\n"
"v_lshlrev_b32 v5, 3, v0\n"
"v_mov_b64 v[6:7], 0\n"
"s_mov_b64 exec, s[42:43]\n"
"global_atomic_or_x2 v[60:61], v5, v[6:7], s[60:61] offset:0 sc0 sc1\n"
"s_mov_b64 exec, -1\n"
"s_waitcnt vmcnt(1)\n"
"v_mov_b32_e64 v62, 1\n"
"ds_write_b32 v3, v62\n"
"s_mov_b64 exec, s[42:43]\n"
"s_waitcnt vmcnt(0)\n"
"v_cmp_ne_u64_e64 vcc, s[32:33], v[60:61]\n"
"s_nop 0\n"
"s_cbranch_vccz .Lfb_AD_6\n"
".Lfb_AP_5:\n"
"global_atomic_or_x2 v[60:61], v5, v[6:7], s[60:61] offset:0 sc0 sc1\n"
"s_waitcnt vmcnt(0)\n"
"v_cmp_ne_u64_e64 vcc, s[32:33], v[60:61]\n"
"s_nop 0\n"
"s_cbranch_vccz .Lfb_AD_6\n"
"s_sleep 0\n"
"s_sub_u32 s31, s31, 1\n"
"s_cbranch_scc0 .Lfb_AP_5\n"
"s_mov_b32 s31, 0\n"
".Lfb_AD_6:\n"
"s_mov_b64 exec, -1\n"
"s_cmp_ge_u32 s22, 2\n"
"s_cbranch_scc1 .Lfb_NH_8\n"
".Lfb_LP_9:\n"
"ds_read_b32 v63, v4 offset:0\n"
"s_waitcnt lgkmcnt(0)\n"
"v_cmp_gt_u32 vcc, 4, v63\n"
"s_nop 0\n"
"s_cbranch_vccz .Lfb_LD_10\n"
"s_sleep 1\n"
"s_sub_u32 s31, s31, 1\n"
"s_cbranch_scc0 .Lfb_LP_9\n"
"s_mov_b32 s31, 0\n"
".Lfb_LD_10:\n"
".Lfb_NH_8:\n"
"global_load_dwordx4 v[16:19], v8, s[66:67] offset:-3072 sc1\n"
"global_load_dwordx4 v[20:23], v9, s[66:67] offset:-3072 sc1\n"
"global_load_dwordx4 v[24:27], v10, s[66:67] offset:-3072 sc1\n"
"global_load_dwordx4 v[28:31], v11, s[66:67] offset:-3072 sc1\n"
"global_load_dwordx4 v[32:35], v8, s[66:67] offset:-2048 sc1\n"
"global_load_dwordx4 v[36:39], v9, s[66:67] offset:-2048 sc1\n"
"global_load_dwordx4 v[40:43], v10, s[66:67] offset:-2048 sc1\n"
"global_load_dwordx4 v[44:47], v11, s[66:67] offset:-2048 sc1\n"
"global_load_dwordx4 v[48:51], v8, s[66:67] offset:-1024 sc1\n"
"global_load_dwordx4 v[52:55], v9, s[66:67] offset:-1024 sc1\n"
"global_load_dwordx4 v[56:59], v10, s[66:67] offset:-1024 sc1\n"
"s_cmp_ge_u32 s22, 2\n"
"s_cbranch_scc1 .Lfb_NH_11\n"
".Lfb_LP_12:\n"
"ds_read_b32 v63, v4 offset:0\n"
"s_waitcnt lgkmcnt(0)\n"
"v_cmp_gt_u32_e64 vcc, 5, v63\n"
"s_nop 0\n"
"s_cbranch_vccz .Lfb_LD_13\n"
"s_sleep 1\n"
"s_sub_u32 s31, s31, 1\n"
"s_cbranch_scc0 .Lfb_LP_12\n"
"s_mov_b32 s31, 0\n"
".Lfb_LD_13:\n"
".Lfb_NH_11:\n"
"s_waitcnt vmcnt(10)\n"
"v_mfma_f32_16x16x32_bf16 v[64:67], v[128:131], v[16:19], 0\n"
"v_mfma_f32_16x16x32_bf16 v[68:71], v[132:135], v[16:19], 0\n"
"v_mfma_f32_16x16x32_bf16 v[72:75], v[136:139], v[16:19], 0\n"
"v_mfma_f32_16x16x32_bf16 v[76:79], v[140:143], v[16:19], 0\n"
"global_load_dwordx4 v[16:19], v11, s[66:67] offset:-1024 sc1\n"
"s_waitcnt vmcnt(10)\n"
"s_nop 0\n"
"v_mfma_f32_16x16x32_bf16 v[80:83], v[128:131], v[20:23], 0\n"
"v_mfma_f32_16x16x32_bf16 v[84:87], v[132:135], v[20:23], 0\n"
"v_mfma_f32_16x16x32_bf16 v[88:91], v[136:139], v[20:23], 0\n"
"v_mfma_f32_16x16x32_bf16 v[92:95], v[140:143], v[20:23], 0\n"
"global_load_dwordx4 v[20:23], v8, s[66:67] offset:0 sc1\n"
"s_waitcnt vmcnt(10)\n"
"s_nop 0\n"
"v_mfma_f32_16x16x32_bf16 v[96:99], v[128:131], v[24:27], 0\n"
"v_mfma_f32_16x16x32_bf16 v[100:103], v[132:135], v[24:27], 0\n"
"v_mfma_f32_16x16x32_bf16 v[104:107], v[136:139], v[24:27], 0\n"
"v_mfma_f32_16x16x32_bf16 v[108:111], v[140:143], v[24:27], 0\n"
"global_load_dwordx4 v[24:27], v9, s[66:67] offset:0 sc1\n"
"s_waitcnt vmcnt(10)\n"
"s_nop 0\n"
"v_mfma_f32_16x16x32_bf16 v[112:115], v[128:131], v[28:31], 0\n"
"v_mfma_f32_16x16x32_bf16 v[116:119], v[132:135], v[28:31], 0\n"
"v_mfma_f32_16x16x32_bf16 v[120:123], v[136:139], v[28:31], 0\n"
"v_mfma_f32_16x16x32_bf16 v[124:127], v[140:143], v[28:31], 0\n"
"global_load_dwordx4 v[28:31], v10, s[66:67] offset:0 sc1\n"
"s_waitcnt vmcnt(10)\n"
"s_nop 0\n"
"v_mfma_f32_16x16x32_bf16 v[64:67], v[144:147], v[32:35], v[64:67]\n"
"ds_read_b64_tr_b16 v[128:129], v1 offset:8192\n"
"v_mfma_f32_16x16x32_bf16 v[68:71], v[148:151], v[32:35], v[68:71]\n"
"ds_read_b64_tr_b16 v[130:131], v1 offset:10240\n"
"v_mfma_f32_16x16x32_bf16 v[72:75], v[152:155], v[32:35], v[72:75]\n"
"ds_read_b64_tr_b16 v[136:137], v2 offset:8192\n"
"v_mfma_f32_16x16x32_bf16 v[76:79], v[156:159], v[32:35], v[76:79]\n"
"global_load_dwordx4 v[32:35], v11, s[66:67] offset:0 sc1\n"
"s_waitcnt vmcnt(10)\n"
"v_mfma_f32_16x16x32_bf16 v[80:83], v[144:147], v[36:39], v[80:83]\n"
"ds_read_b64_tr_b16 v[138:139], v2 offset:10240\n"
"v_mfma_f32_16x16x32_bf16 v[84:87], v[148:151], v[36:39], v[84:87]\n"
"v_xor_b32 v1, 32, v1\n"
"v_mfma_f32_16x16x32_bf16 v[88:91], v[152:155], v[36:39], v[88:91]\n"
"v_xor_b32_e64 v2, 32, v2\n"
"v_mfma_f32_16x16x32_bf16 v[92:95], v[156:159], v[36:39], v[92:95]\n"
"global_load_dwordx4 v[36:39], v8, s[66:67] offset:1024 sc1\n"
"s_waitcnt vmcnt(10)\n"
"s_nop 0\n"
"v_mfma_f32_16x16x32_bf16 v[96:99], v[144:147], v[40:43], v[96:99]\n"
"ds_read_b64_tr_b16 v[132:133], v1 offset:8192\n"
"v_mfma_f32_16x16x32_bf16 v[100:103], v[148:151], v[40:43], v[100:103]\n"
"ds_read_b64_tr_b16 v[134:135], v1 offset:10240\n"
"v_mfma_f32_16x16x32_bf16 v[104:107], v[152:155], v[40:43], v[104:107]\n"
"ds_read_b64_tr_b16 v[140:141], v2 offset:8192\n"
"v_mfma_f32_16x16x32_bf16 v[108:111], v[156:159], v[40:43], v[108:111]\n"
"global_load_dwordx4 v[40:43], v9, s[66:67] offset:1024 sc1\n"
"s_mov_b64 exec, s[44:45]\n"
"s_nop 0\n"
"global_atomic_or_x2 v[60:61], v5, v[6:7], s[60:61] offset:128 sc0 sc1\n"
"s_mov_b64 exec, -1\n"
"s_waitcnt vmcnt(11)\n"
"v_mfma_f32_16x16x32_bf16 v[112:115], v[144:147], v[44:47], v[112:115]\n"
"ds_read_b64_tr_b16 v[142:143], v2 offset:10240\n"
"v_mfma_f32_16x16x32_bf16 v[116:119], v[148:151], v[44:47], v[116:119]\n"
"v_mfma_f32_16x16x32_bf16 v[120:123], v[152:155], v[44:47], v[120:123]\n"
"v_mfma_f32_16x16x32_bf16 v[124:127], v[156:159], v[44:47], v[124:127]\n"
"global_load_dwordx4 v[44:47], v10, s[66:67] offset:1024 sc1\n"
"s_waitcnt vmcnt(11)\n"
"s_nop 0\n"
"v_mfma_f32_16x16x32_bf16 v[64:67], v[160:163], v[48:51], v[64:67]\n"
"ds_read_b64_tr_b16 v[148:149], v1 offset:0\n"
"v_mfma_f32_16x16x32_bf16 v[68:71], v[164:167], v[48:51], v[68:71]\n"
"ds_read_b64_tr_b16 v[150:151], v1 offset:2048\n"
"v_mfma_f32_16x16x32_bf16 v[72:75], v[168:171], v[48:51], v[72:75]\n"
"ds_read_b64_tr_b16 v[156:157], v2 offset:0\n"
"v_mfma_f32_16x16x32_bf16 v[76:79], v[172:175], v[48:51], v[76:79]\n"
"global_load_dwordx4 v[48:51], v11, s[66:67] offset:1024 sc1\n"
"s_waitcnt vmcnt(11)\n"
"v_mfma_f32_16x16x32_bf16 v[80:83], v[160:163], v[52:55], v[80:83]\n"
"ds_read_b64_tr_b16 v[158:159], v2 offset:2048\n"
"v_mfma_f32_16x16x32_bf16 v[84:87], v[164:167], v[52:55], v[84:87]\n"
"v_xor_b32 v1, 32, v1\n"
"v_mfma_f32_16x16x32_bf16 v[88:91], v[168:171], v[52:55], v[88:91]\n"
"v_xor_b32_e64 v2, 32, v2\n"
"v_mfma_f32_16x16x32_bf16 v[92:95], v[172:175], v[52:55], v[92:95]\n"
"global_load_dwordx4 v[52:55], v8, s[66:67] offset:2048 sc1\n"
"s_waitcnt vmcnt(11)\n"
"s_nop 0\n"
"v_mfma_f32_16x16x32_bf16 v[96:99], v[160:163], v[56:59], v[96:99]\n"
"ds_read_b64_tr_b16 v[144:145], v1 offset:0\n"
"v_mfma_f32_16x16x32_bf16 v[100:103], v[164:167], v[56:59], v[100:103]\n"
"ds_read_b64_tr_b16 v[146:147], v1 offset:2048\n"
"v_mfma_f32_16x16x32_bf16 v[104:107], v[168:171], v[56:59], v[104:107]\n"
"ds_read_b64_tr_b16 v[152:153], v2 offset:0\n"
"v_mfma_f32_16x16x32_bf16 v[108:111], v[172:175], v[56:59], v[108:111]\n"
"global_load_dwordx4 v[56:59], v9, s[66:67] offset:2048 sc1\n"
"s_waitcnt vmcnt(11)\n"
"s_nop 0\n"
"v_mfma_f32_16x16x32_bf16 v[112:115], v[160:163], v[16:19], v[112:115]\n"
"ds_read_b64_tr_b16 v[154:155], v2 offset:2048\n"
"v_mfma_f32_16x16x32_bf16 v[116:119], v[164:167], v[16:19], v[116:119]\n"
"v_mfma_f32_16x16x32_bf16 v[120:123], v[168:171], v[16:19], v[120:123]\n"
"v_mfma_f32_16x16x32_bf16 v[124:127], v[172:175], v[16:19], v[124:127]\n"
"global_load_dwordx4 v[16:19], v10, s[66:67] offset:2048 sc1\n"
"s_waitcnt vmcnt(11)\n"
"s_nop 0\n"
"v_mfma_f32_16x16x32_bf16 v[64:67], v[176:179], v[20:23], v[64:67]\n"
"ds_read_b64_tr_b16 v[160:161], v1 offset:4096\n"
"v_mfma_f32_16x16x32_bf16 v[68:71], v[180:183], v[20:23], v[68:71]\n"
"ds_read_b64_tr_b16 v[162:163], v1 offset:6144\n"
"v_mfma_f32_16x16x32_bf16 v[72:75], v[184:187], v[20:23], v[72:75]\n"
"ds_read_b64_tr_b16 v[168:169], v2 offset:4096\n"
"v_mfma_f32_16x16x32_bf16 v[76:79], v[188:191], v[20:23], v[76:79]\n"
"global_load_dwordx4 v[20:23], v11, s[66:67] offset:2048 sc1\n"
"s_waitcnt vmcnt(11)\n"
"v_mfma_f32_16x16x32_bf16 v[80:83], v[176:179], v[24:27], v[80:83]\n"
"ds_read_b64_tr_b16 v[170:171], v2 offset:6144\n"
"v_mfma_f32_16x16x32_bf16 v[84:87], v[180:183], v[24:27], v[84:87]\n"
"v_xor_b32 v1, 32, v1\n"
"v_mfma_f32_16x16x32_bf16 v[88:91], v[184:187], v[24:27], v[88:91]\n"
"v_xor_b32_e64 v2, 32, v2\n"
"v_mfma_f32_16x16x32_bf16 v[92:95], v[188:191], v[24:27], v[92:95]\n"
"global_load_dwordx4 v[24:27], v8, s[66:67] offset:3072 sc1\n"
"s_waitcnt vmcnt(11)\n"
"s_nop 0\n"
"v_mfma_f32_16x16x32_bf16 v[96:99], v[176:179], v[28:31], v[96:99]\n"
"ds_read_b64_tr_b16 v[164:165], v1 offset:4096\n"
"v_mfma_f32_16x16x32_bf16 v[100:103], v[180:183], v[28:31], v[100:103]\n"
"ds_read_b64_tr_b16 v[166:167], v1 offset:6144\n"
"v_mfma_f32_16x16x32_bf16 v[104:107], v[184:187], v[28:31], v[104:107]\n"
"ds_read_b64_tr_b16 v[172:173], v2 offset:4096\n"
"v_mfma_f32_16x16x32_bf16 v[108:111], v[188:191], v[28:31], v[108:111]\n"
"global_load_dwordx4 v[28:31], v9, s[66:67] offset:3072 sc1\n"
"s_waitcnt vmcnt(11)\n"
"s_nop 0\n"
"v_mfma_f32_16x16x32_bf16 v[112:115], v[176:179], v[32:35], v[112:115]\n"
"ds_read_b64_tr_b16 v[174:175], v2 offset:6144\n"
"v_mfma_f32_16x16x32_bf16 v[116:119], v[180:183], v[32:35], v[116:119]\n"
"v_mfma_f32_16x16x32_bf16 v[120:123], v[184:187], v[32:35], v[120:123]\n"
"v_mfma_f32_16x16x32_bf16 v[124:127], v[188:191], v[32:35], v[124:127]\n"
"global_load_dwordx4 v[32:35], v10, s[66:67] offset:3072 sc1\n"
"s_waitcnt vmcnt(11)\n"
"s_nop 0\n"
"v_mfma_f32_16x16x32_bf16 v[64:67], v[192:195], v[36:39], v[64:67]\n"
"v_mfma_f32_16x16x32_bf16 v[68:71], v[196:199], v[36:39], v[68:71]\n"
"v_mfma_f32_16x16x32_bf16 v[72:75], v[200:203], v[36:39], v[72:75]\n"
"v_mfma_f32_16x16x32_bf16 v[76:79], v[204:207], v[36:39], v[76:79]\n"
"global_load_dwordx4 v[36:39], v11, s[66:67] offset:3072 sc1\n"
"s_waitcnt vmcnt(11)\n"
"s_nop 0\n"
"v_mfma_f32_16x16x32_bf16 v[80:83], v[192:195], v[40:43], v[80:83]\n"
"v_mfma_f32_16x16x32_bf16 v[84:87], v[196:199], v[40:43], v[84:87]\n"
"v_mfma_f32_16x16x32_bf16 v[88:91], v[200:203], v[40:43], v[88:91]\n"
"v_mfma_f32_16x16x32_bf16 v[92:95], v[204:207], v[40:43], v[92:95]\n"
"s_mov_b64 exec, s[44:45]\n"
"s_waitcnt vmcnt(10)\n"
"v_cmp_ne_u64_e64 vcc, s[32:33], v[60:61]\n"
"s_nop 0\n"
"s_cbranch_vccz .Lfb_AD_15\n"
".Lfb_AP_14:\n"
"global_atomic_or_x2 v[60:61], v5, v[6:7], s[60:61] offset:128 sc0 sc1\n"
"s_waitcnt vmcnt(0)\n"
"v_cmp_ne_u64 vcc, s[32:33], v[60:61]\n"
"s_nop 0\n"
"s_cbranch_vccz .Lfb_AD_15\n"
"s_sleep 0\n"
"s_sub_u32 s31, s31, 1\n"
"s_cbranch_scc0 .Lfb_AP_14\n"
"s_mov_b32 s31, 0\n"
".Lfb_AD_15:\n"
"s_mov_b64 exec, -1\n"
"global_load_dwordx4 v[40:43], v12, s[68:69] offset:-4096 sc1\n"
"s_waitcnt vmcnt(10)\n"
"v_mfma_f32_16x16x32_bf16 v[96:99], v[192:195], v[44:47], v[96:99]\n"
"v_mfma_f32_16x16x32_bf16 v[100:103], v[196:199], v[44:47], v[100:103]\n"
"v_mfma_f32_16x16x32_bf16 v[104:107], v[200:203], v[44:47], v[104:107]\n"
"v_mfma_f32_16x16x32_bf16 v[108:111], v[204:207], v[44:47], v[108:111]\n"
"global_load_dwordx4 v[44:47], v12, s[68:69] offset:-2048 sc1\n"
"v_mov_b32_e64 v62, 2\n"
"ds_write_b32 v3, v62\n"
"s_waitcnt vmcnt(10)\n"
"s_nop 0\n"
"v_mfma_f32_16x16x32_bf16 v[112:115], v[192:195], v[48:51], v[112:115]\n"
"v_mfma_f32_16x16x32_bf16 v[116:119], v[196:199], v[48:51], v[116:119]\n"
"v_mfma_f32_16x16x32_bf16 v[120:123], v[200:203], v[48:51], v[120:123]\n"
"v_mfma_f32_16x16x32_bf16 v[124:127], v[204:207], v[48:51], v[124:127]\n"
"global_load_dwordx4 v[48:51], v12, s[68:69] offset:0 sc1\n"
"s_waitcnt vmcnt(10)\n"
"s_nop 0\n"
"v_mfma_f32_16x16x32_bf16 v[64:67], v[208:211], v[52:55], v[64:67]\n"
"v_mfma_f32_16x16x32_bf16 v[68:71], v[212:215], v[52:55], v[68:71]\n"
"v_mfma_f32_16x16x32_bf16 v[72:75], v[216:219], v[52:55], v[72:75]\n"
"v_mfma_f32_16x16x32_bf16 v[76:79], v[220:223], v[52:55], v[76:79]\n"
"global_load_dwordx4 v[52:55], v12, s[68:69] offset:2048 sc1\n"
"s_waitcnt vmcnt(10)\n"
"s_nop 0\n"
"v_mfma_f32_16x16x32_bf16 v[80:83], v[208:211], v[56:59], v[80:83]\n"
"v_mfma_f32_16x16x32_bf16 v[84:87], v[212:215], v[56:59], v[84:87]\n"
"v_mfma_f32_16x16x32_bf16 v[88:91], v[216:219], v[56:59], v[88:91]\n"
"v_mfma_f32_16x16x32_bf16 v[92:95], v[220:223], v[56:59], v[92:95]\n"
"global_load_dwordx4 v[56:59], v13, s[68:69] offset:-4096 sc1\n"
"s_waitcnt vmcnt(10)\n"
"s_nop 0\n"
"v_mfma_f32_16x16x32_bf16 v[96:99], v[208:211], v[16:19], v[96:99]\n"
"v_mfma_f32_16x16x32_bf16 v[100:103], v[212:215], v[16:19], v[100:103]\n"
"v_mfma_f32_16x16x32_bf16 v[104:107], v[216:219], v[16:19], v[104:107]\n"
"v_mfma_f32_16x16x32_bf16 v[108:111], v[220:223], v[16:19], v[108:111]\n"
"global_load_dwordx4 v[16:19], v13, s[68:69] offset:-2048 sc1\n"
"s_waitcnt vmcnt(10)\n"
"s_nop 0\n"
"v_mfma_f32_16x16x32_bf16 v[112:115], v[208:211], v[20:23], v[112:115]\n"
"v_mfma_f32_16x16x32_bf16 v[116:119], v[212:215], v[20:23], v[116:119]\n"
"v_mfma_f32_16x16x32_bf16 v[120:123], v[216:219], v[20:23], v[120:123]\n"
"v_mfma_f32_16x16x32_bf16 v[124:127], v[220:223], v[20:23], v[124:127]\n"
"global_load_dwordx4 v[20:23], v13, s[68:69] offset:0 sc1\n"
"s_waitcnt vmcnt(10)\n"
"s_nop 0\n"
"v_mfma_f32_16x16x32_bf16 v[64:67], v[224:227], v[24:27], v[64:67]\n"
"v_mfma_f32_16x16x32_bf16 v[68:71], v[228:231], v[24:27], v[68:71]\n"
"v_mfma_f32_16x16x32_bf16 v[72:75], v[232:235], v[24:27], v[72:75]\n"
"v_mfma_f32_16x16x32_bf16 v[76:79], v[236:239], v[24:27], v[76:79]\n"
"global_load_dwordx4 v[24:27], v13, s[68:69] offset:2048 sc1\n"
"s_waitcnt vmcnt(10)\n"
"s_nop 0\n"
"v_mfma_f32_16x16x32_bf16 v[80:83], v[224:227], v[28:31], v[80:83]\n"
"v_mfma_f32_16x16x32_bf16 v[84:87], v[228:231], v[28:31], v[84:87]\n"
"v_mfma_f32_16x16x32_bf16 v[88:91], v[232:235], v[28:31], v[88:91]\n"
"v_mfma_f32_16x16x32_bf16 v[92:95], v[236:239], v[28:31], v[92:95]\n"
"global_load_dwordx4 v[28:31], v14, s[68:69] offset:-4096 sc1\n"
"s_waitcnt vmcnt(10)\n"
"s_nop 0\n"
"v_mfma_f32_16x16x32_bf16 v[96:99], v[224:227], v[32:35], v[96:99]\n"
"v_mfma_f32_16x16x32_bf16 v[100:103], v[228:231], v[32:35], v[100:103]\n"
"v_mfma_f32_16x16x32_bf16 v[104:107], v[232:235], v[32:35], v[104:107]\n"
"v_mfma_f32_16x16x32_bf16 v[108:111], v[236:239], v[32:35], v[108:111]\n"
"global_load_dwordx4 v[32:35], v14, s[68:69] offset:-2048 sc1\n"
"s_waitcnt vmcnt(10)\n"
"s_nop 0\n"
"v_mfma_f32_16x16x32_bf16 v[112:115], v[224:227], v[36:39], v[112:115]\n"
"v_mfma_f32_16x16x32_bf16 v[116:119], v[228:231], v[36:39], v[116:119]\n"
"v_mfma_f32_16x16x32_bf16 v[120:123], v[232:235], v[36:39], v[120:123]\n"
"v_mfma_f32_16x16x32_bf16 v[124:127], v[236:239], v[36:39], v[124:127]\n"
"global_load_dwordx4 v[36:39], v14, s[68:69] offset:0 sc1\n"
"s_waitcnt vmcnt(10)\n"
"s_waitcnt lgkmcnt(0)\n"
"s_cmp_lg_u32 s20, 3\n"
"s_cselect_b64 vcc, -1, 0\n"
"v_mfma_f32_16x16x32_bf16 v[64:67], v[240:243], v[40:43], v[64:67]\n"
"v_mfma_f32_16x16x32_bf16 v[68:71], v[244:247], v[40:43], v[68:71]\n"
"v_mfma_f32_16x16x32_bf16 v[72:75], v[248:251], v[40:43], v[72:75]\n"
"v_mfma_f32_16x16x32_bf16 v[76:79], v[252:255], v[40:43], v[76:79]\n"
"global_load_dwordx4 v[40:43], v14, s[68:69] offset:2048 sc1\n"
"v_lshlrev_b32 v5, 4, v0\n"
"v_add_u32 v8, s74, v5\n"
"s_add_u32 s34, s20, 3\n"
"s_and_b32 s34, s34, 3\n"
"s_mul_i32 s34, s34, 12288\n"
"s_add_u32 s34, s34, 98304\n"
"v_add_u32_e64 v9, s34, v5\n"
"s_add_u32 s34, s20, 2\n"
"s_and_b32 s34, s34, 3\n"
"s_mul_i32 s34, s34, 12288\n"
"s_add_u32 s34, s34, 102400\n"
"v_add_u32_e64 v10, s34, v5\n"
"s_add_u32 s34, s20, 1\n"
"s_and_b32 s34, s34, 3\n"
"s_mul_i32 s34, s34, 12288\n"
"s_add_u32 s34, s34, 106496\n"
"v_add_u32 v11, s34, v5\n"
"v_subrev_u32 v4, 16, v4\n"
"s_mov_b32 s38, 0xbfb8aa3b\n"
"s_waitcnt vmcnt(10)\n"
"s_nop 0\n"
"v_mfma_f32_16x16x32_bf16 v[64:67], v[128:131], v[44:47], v[64:67]\n"
"v_mfma_f32_16x16x32_bf16 v[68:71], v[132:135], v[44:47], v[68:71]\n"
"v_mfma_f32_16x16x32_bf16 v[72:75], v[136:139], v[44:47], v[72:75]\n"
"v_mfma_f32_16x16x32_bf16 v[76:79], v[140:143], v[44:47], v[76:79]\n"
"global_load_dwordx4 v[44:47], v15, s[68:69] offset:-4096 sc1\n"
"s_waitcnt vmcnt(10)\n"
"s_nop 0\n"
"v_mfma_f32_16x16x32_bf16 v[64:67], v[144:147], v[48:51], v[64:67]\n"
"v_mfma_f32_16x16x32_bf16 v[68:71], v[148:151], v[48:51], v[68:71]\n"
"v_mfma_f32_16x16x32_bf16 v[72:75], v[152:155], v[48:51], v[72:75]\n"
"v_mfma_f32_16x16x32_bf16 v[76:79], v[156:159], v[48:51], v[76:79]\n"
"global_load_dwordx4 v[48:51], v15, s[68:69] offset:-2048 sc1\n"
"s_waitcnt vmcnt(10)\n"
"s_cbranch_vccz .Lfb_SK_17\n"
"v_mfma_f32_16x16x32_bf16 v[64:67], v[160:163], v[52:55], v[64:67]\n"
"v_mfma_f32_16x16x32_bf16 v[68:71], v[164:167], v[52:55], v[68:71]\n"
"v_mfma_f32_16x16x32_bf16 v[72:75], v[168:171], v[52:55], v[72:75]\n"
"v_mfma_f32_16x16x32_bf16 v[76:79], v[172:175], v[52:55], v[76:79]\n"
".Lfb_SK_17:\n"
"global_load_dwordx4 v[52:55], v15, s[68:69] offset:0 sc1\n"
"s_nop 0\n"
"s_nop 3\n"
"ds_write_b128 v8, v[64:67] offset:0\n"
"ds_write_b128 v8, v[68:71] offset:1024\n"
"ds_write_b128 v8, v[72:75] offset:2048\n"
"ds_write_b128 v8, v[76:79] offset:3072\n"
"s_waitcnt vmcnt(10)\n"
"s_nop 0\n"
"v_mfma_f32_16x16x32_bf16 v[80:83], v[240:243], v[56:59], v[80:83]\n"
"v_mfma_f32_16x16x32_bf16 v[84:87], v[244:247], v[56:59], v[84:87]\n"
"v_mfma_f32_16x16x32_bf16 v[88:91], v[248:251], v[56:59], v[88:91]\n"
"v_mfma_f32_16x16x32_bf16 v[92:95], v[252:255], v[56:59], v[92:95]\n"
"global_load_dwordx4 v[56:59], v15, s[68:69] offset:2048 sc1\n"
"v_mov_b32_e64 v62, 3\n"
"ds_write_b32 v3, v62\n"
"s_waitcnt vmcnt(10)\n"
"s_nop 0\n"
"v_mfma_f32_16x16x32_bf16 v[80:83], v[128:131], v[16:19], v[80:83]\n"
"v_mfma_f32_16x16x32_bf16 v[84:87], v[132:135], v[16:19], v[84:87]\n"
"v_mfma_f32_16x16x32_bf16 v[88:91], v[136:139], v[16:19], v[88:91]\n"
"v_mfma_f32_16x16x32_bf16 v[92:95], v[140:143], v[16:19], v[92:95]\n"
"s_waitcnt vmcnt(9)\n"
"s_nop 0\n"
"v_mfma_f32_16x16x32_bf16 v[80:83], v[144:147], v[20:23], v[80:83]\n"
"v_mfma_f32_16x16x32_bf16 v[84:87], v[148:151], v[20:23], v[84:87]\n"
"v_mfma_f32_16x16x32_bf16 v[88:91], v[152:155], v[20:23], v[88:91]\n"
"v_mfma_f32_16x16x32_bf16 v[92:95], v[156:159], v[20:23], v[92:95]\n"
"s_waitcnt vmcnt(8)\n"
"s_cbranch_vccz .Lfb_SK_18\n"
"v_mfma_f32_16x16x32_bf16 v[80:83], v[160:163], v[24:27], v[80:83]\n"
"v_mfma_f32_16x16x32_bf16 v[84:87], v[164:167], v[24:27], v[84:87]\n"
"v_mfma_f32_16x16x32_bf16 v[88:91], v[168:171], v[24:27], v[88:91]\n"
"v_mfma_f32_16x16x32_bf16 v[92:95], v[172:175], v[24:27], v[92:95]\n"
".Lfb_SK_18:\n"
"s_nop 0\n"
"s_nop 3\n"
"ds_write_b128 v8, v[80:83] offset:4096\n"
"ds_write_b128 v8, v[84:87] offset:5120\n"
"ds_write_b128 v8, v[88:91] offset:6144\n"
"ds_write_b128 v8, v[92:95] offset:7168\n"
"s_waitcnt vmcnt(7)\n"
"s_nop 0\n"
"v_mfma_f32_16x16x32_bf16 v[96:99], v[240:243], v[28:31], v[96:99]\n"
"v_mfma_f32_16x16x32_bf16 v[100:103], v[244:247], v[28:31], v[100:103]\n"
"v_mfma_f32_16x16x32_bf16 v[104:107], v[248:251], v[28:31], v[104:107]\n"
"v_mfma_f32_16x16x32_bf16 v[108:111], v[252:255], v[28:31], v[108:111]\n"
"s_waitcnt vmcnt(6)\n"
"s_nop 0\n"
"v_mfma_f32_16x16x32_bf16 v[96:99], v[128:131], v[32:35], v[96:99]\n"
"v_mfma_f32_16x16x32_bf16 v[100:103], v[132:135], v[32:35], v[100:103]\n"
"v_mfma_f32_16x16x32_bf16 v[104:107], v[136:139], v[32:35], v[104:107]\n"
"v_mfma_f32_16x16x32_bf16 v[108:111], v[140:143], v[32:35], v[108:111]\n"
"s_waitcnt vmcnt(5)\n"
"s_nop 0\n"
"v_mfma_f32_16x16x32_bf16 v[96:99], v[144:147], v[36:39], v[96:99]\n"
"v_mfma_f32_16x16x32_bf16 v[100:103], v[148:151], v[36:39], v[100:103]\n"
"v_mfma_f32_16x16x32_bf16 v[104:107], v[152:155], v[36:39], v[104:107]\n"
"v_mfma_f32_16x16x32_bf16 v[108:111], v[156:159], v[36:39], v[108:111]\n"
"s_waitcnt vmcnt(4)\n"
"s_cbranch_vccz .Lfb_SK_19\n"
"v_mfma_f32_16x16x32_bf16 v[96:99], v[160:163], v[40:43], v[96:99]\n"
"v_mfma_f32_16x16x32_bf16 v[100:103], v[164:167], v[40:43], v[100:103]\n"
"v_mfma_f32_16x16x32_bf16 v[104:107], v[168:171], v[40:43], v[104:107]\n"
"v_mfma_f32_16x16x32_bf16 v[108:111], v[172:175], v[40:43], v[108:111]\n"
".Lfb_SK_19:\n"
"s_nop 0\n"
"s_nop 3\n"
"ds_write_b128 v8, v[96:99] offset:8192\n"
"ds_write_b128 v8, v[100:103] offset:9216\n"
"ds_write_b128 v8, v[104:107] offset:10240\n"
"ds_write_b128 v8, v[108:111] offset:11264\n"
"v_mov_b32_e64 v62, 4\n"
"ds_write_b32 v3, v62\n"
"s_setprio 2\n"
"s_waitcnt vmcnt(3)\n"
"v_mfma_f32_16x16x32_bf16 v[112:115], v[240:243], v[44:47], v[112:115]\n"
"v_mfma_f32_16x16x32_bf16 v[116:119], v[244:247], v[44:47], v[116:119]\n"
"v_mfma_f32_16x16x32_bf16 v[120:123], v[248:251], v[44:47], v[120:123]\n"
"v_mfma_f32_16x16x32_bf16 v[124:127], v[252:255], v[44:47], v[124:127]\n"
"s_waitcnt vmcnt(2)\n"
"s_nop 0\n"
"v_mfma_f32_16x16x32_bf16 v[112:115], v[128:131], v[48:51], v[112:115]\n"
"v_mfma_f32_16x16x32_bf16 v[116:119], v[132:135], v[48:51], v[116:119]\n"
"v_mfma_f32_16x16x32_bf16 v[120:123], v[136:139], v[48:51], v[120:123]\n"
"v_mfma_f32_16x16x32_bf16 v[124:127], v[140:143], v[48:51], v[124:127]\n"
"s_waitcnt vmcnt(1)\n"
"s_nop 0\n"
"v_mfma_f32_16x16x32_bf16 v[112:115], v[144:147], v[52:55], v[112:115]\n"
"v_mfma_f32_16x16x32_bf16 v[116:119], v[148:151], v[52:55], v[116:119]\n"
"v_mfma_f32_16x16x32_bf16 v[120:123], v[152:155], v[52:55], v[120:123]\n"
"v_mfma_f32_16x16x32_bf16 v[124:127], v[156:159], v[52:55], v[124:127]\n"
"s_waitcnt vmcnt(0)\n"
"s_cbranch_vccz .Lfb_SK_20\n"
"v_mfma_f32_16x16x32_bf16 v[112:115], v[160:163], v[56:59], v[112:115]\n"
"v_mfma_f32_16x16x32_bf16 v[116:119], v[164:167], v[56:59], v[116:119]\n"
"v_mfma_f32_16x16x32_bf16 v[120:123], v[168:171], v[56:59], v[120:123]\n"
"v_mfma_f32_16x16x32_bf16 v[124:127], v[172:175], v[56:59], v[124:127]\n"
".Lfb_SK_20:\n"
".Lfb_LP_21:\n"
"ds_read_b32 v60, v4 offset:0\n"
"s_waitcnt lgkmcnt(0)\n"
"v_cmp_gt_u32 vcc, 4, v60\n"
"s_nop 0\n"
"s_cbranch_vccz .Lfb_LD_22\n"
"s_sleep 0\n"
"s_sub_u32 s31, s31, 1\n"
"s_cbranch_scc0 .Lfb_LP_21\n"
"s_mov_b32 s31, 0\n"
".Lfb_LD_22:\n"
"ds_read_b128 v[176:179], v9 offset:0\n"
"ds_read_b128 v[180:183], v9 offset:1024\n"
"ds_read_b128 v[184:187], v9 offset:2048\n"
"ds_read_b128 v[188:191], v9 offset:3072\n"
"ds_read_b128 v[192:195], v10 offset:0\n"
"ds_read_b128 v[196:199], v10 offset:1024\n"
"ds_read_b128 v[200:203], v10 offset:2048\n"
"ds_read_b128 v[204:207], v10 offset:3072\n"
"ds_read_b128 v[208:211], v11 offset:0\n"
"ds_read_b128 v[212:215], v11 offset:1024\n"
"ds_read_b128 v[216:219], v11 offset:2048\n"
"ds_read_b128 v[220:223], v11 offset:3072\n"
"s_waitcnt lgkmcnt(8)\n"
"v_add_f32 v112, v112, v176\n"
"v_add_f32 v113, v113, v177\n"
"v_add_f32 v114, v114, v178\n"
"v_add_f32 v115, v115, v179\n"
"v_add_f32 v116, v116, v180\n"
"v_add_f32 v117, v117, v181\n"
"v_add_f32 v118, v118, v182\n"
"v_add_f32 v119, v119, v183\n"
"v_add_f32 v120, v120, v184\n"
"v_add_f32 v121, v121, v185\n"
"v_add_f32 v122, v122, v186\n"
"v_add_f32 v123, v123, v187\n"
"v_add_f32 v124, v124, v188\n"
"v_add_f32 v125, v125, v189\n"
"v_add_f32 v126, v126, v190\n"
"v_add_f32 v127, v127, v191\n"
"s_waitcnt lgkmcnt(4)\n"
"v_add_f32 v112, v112, v192\n"
"v_add_f32 v113, v113, v193\n"
"v_add_f32 v114, v114, v194\n"
"v_add_f32 v115, v115, v195\n"
"v_add_f32 v116, v116, v196\n"
"v_add_f32 v117, v117, v197\n"
"v_add_f32 v118, v118, v198\n"
"v_add_f32 v119, v119, v199\n"
"v_add_f32 v120, v120, v200\n"
"v_add_f32 v121, v121, v201\n"
"v_add_f32 v122, v122, v202\n"
"v_add_f32 v123, v123, v203\n"
"v_add_f32 v124, v124, v204\n"
"v_add_f32 v125, v125, v205\n"
"v_add_f32 v126, v126, v206\n"
"v_add_f32 v127, v127, v207\n"
"s_waitcnt lgkmcnt(0)\n"
"v_add_f32 v112, v112, v208\n"
"v_add_f32 v113, v113, v209\n"
"v_add_f32 v114, v114, v210\n"
"v_add_f32 v115, v115, v211\n"
"v_add_f32 v116, v116, v212\n"
"v_add_f32 v117, v117, v213\n"
"v_add_f32 v118, v118, v214\n"
"v_add_f32 v119, v119, v215\n"
"v_add_f32 v120, v120, v216\n"
"v_add_f32 v121, v121, v217\n"
"v_add_f32 v122, v122, v218\n"
"v_add_f32 v123, v123, v219\n"
"v_add_f32 v124, v124, v220\n"
"v_add_f32 v125, v125, v221\n"
"v_add_f32 v126, v126, v222\n"
"v_add_f32 v127, v127, v223\n"
"v_mul_f32 v64, s38, v112\n"
"v_mul_f32 v65, s38, v113\n"
"v_mul_f32 v66, s38, v114\n"
"v_mul_f32 v67, s38, v115\n"
"v_exp_f32 v64, v64\n"
"v_exp_f32 v65, v65\n"
"v_exp_f32 v66, v66\n"
"v_exp_f32 v67, v67\n"
"v_add_f32 v64, 1.0, v64\n"
"v_add_f32 v65, 1.0, v65\n"
"v_add_f32 v66, 1.0, v66\n"
"v_add_f32 v67, 1.0, v67\n"
"v_rcp_f32 v64, v64\n"
"v_rcp_f32 v65, v65\n"
"v_rcp_f32 v66, v66\n"
"v_rcp_f32 v67, v67\n"
"v_mul_f32 v112, v112, v64\n"
"v_mul_f32 v113, v113, v65\n"
"v_mul_f32 v114, v114, v66\n"
"v_mul_f32_e64 v115, v115, v67\n"
"ds_write_b128 v9, v[112:115] offset:0\n"
"v_mul_f32 v68, s38, v116\n"
"v_mul_f32 v69, s38, v117\n"
"v_mul_f32 v70, s38, v118\n"
"v_mul_f32 v71, s38, v119\n"
"v_exp_f32 v68, v68\n"
"v_exp_f32 v69, v69\n"
"v_exp_f32 v70, v70\n"
"v_exp_f32 v71, v71\n"
"v_add_f32 v68, 1.0, v68\n"
"v_add_f32 v69, 1.0, v69\n"
"v_add_f32 v70, 1.0, v70\n"
"v_add_f32 v71, 1.0, v71\n"
"v_rcp_f32 v68, v68\n"
"v_rcp_f32 v69, v69\n"
"v_rcp_f32 v70, v70\n"
"v_rcp_f32 v71, v71\n"
"v_mul_f32 v116, v116, v68\n"
"v_mul_f32 v117, v117, v69\n"
"v_mul_f32 v118, v118, v70\n"
"v_mul_f32 v119, v119, v71\n"
"ds_write_b128 v9, v[116:119] offset:1024\n"
"v_mul_f32 v72, s38, v120\n"
"v_mul_f32 v73, s38, v121\n"
"v_mul_f32 v74, s38, v122\n"
"v_mul_f32 v75, s38, v123\n"
"v_exp_f32 v72, v72\n"
"v_exp_f32 v73, v73\n"
"v_exp_f32 v74, v74\n"
"v_exp_f32 v75, v75\n"
"v_add_f32 v72, 1.0, v72\n"
"v_add_f32 v73, 1.0, v73\n"
"v_add_f32 v74, 1.0, v74\n"
"v_add_f32 v75, 1.0, v75\n"
"v_rcp_f32 v72, v72\n"
"v_rcp_f32 v73, v73\n"
"v_rcp_f32 v74, v74\n"
"v_rcp_f32 v75, v75\n"
"v_mul_f32 v120, v120, v72\n"
"v_mul_f32 v121, v121, v73\n"
"v_mul_f32 v122, v122, v74\n"
"v_mul_f32 v123, v123, v75\n"
"ds_write_b128 v9, v[120:123] offset:2048\n"
"v_mul_f32 v76, s38, v124\n"
"v_mul_f32 v77, s38, v125\n"
"v_mul_f32 v78, s38, v126\n"
"v_mul_f32 v79, s38, v127\n"
"v_exp_f32 v76, v76\n"
"v_exp_f32 v77, v77\n"
"v_exp_f32 v78, v78\n"
"v_exp_f32 v79, v79\n"
"v_add_f32 v76, 1.0, v76\n"
"v_add_f32 v77, 1.0, v77\n"
"v_add_f32 v78, 1.0, v78\n"
"v_add_f32 v79, 1.0, v79\n"
"v_rcp_f32 v76, v76\n"
"v_rcp_f32 v77, v77\n"
"v_rcp_f32 v78, v78\n"
"v_rcp_f32 v79, v79\n"
"v_mul_f32 v124, v124, v76\n"
"v_mul_f32 v125, v125, v77\n"
"v_mul_f32 v126, v126, v78\n"
"v_mul_f32 v127, v127, v79\n"
"ds_write_b128 v9, v[124:127] offset:3072\n"
"v_mov_b32_e64 v62, 5\n"
"ds_write_b32 v3, v62\n"
"s_endpgm\n"
".Lfb_H_GR:\n"
"s_sleep 2\n"
"s_lshl_b32 s34, s20, 11\n"
"s_mul_i32 s35, s20, 2056\n"
"s_add_u32 m0, s35, 147456\n"
"s_nop 0\n"
"global_load_lds_dwordx4 v80, s[64:65] sc1\n"
"s_add_u32 m0, s35, 148480\n"
"s_nop 0\n"
"global_load_lds_dwordx4 v81, s[64:65] sc1\n"
"s_cmp_eq_u32 s20, 3\n"
"s_cbranch_scc1 .Lfb_TL_23\n"
"s_add_u32 m0, s34, 155680\n"
"s_nop 0\n"
"global_load_lds_dwordx4 v82, s[76:77] sc1\n"
"s_add_u32 m0, s34, 156704\n"
"s_nop 0\n"
"global_load_lds_dwordx4 v83, s[76:77] sc1\n"
".Lfb_TL_23:\n"
"s_sub_u32 s31, s31, 1\n"
"s_waitcnt vmcnt(0)\n"
"s_cbranch_scc0 .Lfb_H_GC\n"
"s_mov_b32 s31, 0\n"
"s_branch .Lfb_H_GO\n"
".Lfb_H_IDLE:\n"
"s_load_dwordx2 s[32:33], s[18:19], 0x0\n"
"s_and_b32 s34, s26, 3\n"
"s_and_b32 s29, s2, 7\n"
"s_lshl2_add_u32 s29, s29, s34\n"
"s_lshr_b32 s30, s26, 2\n"
"s_sub_u32 s34, 0x1ff, s14\n"
"s_bfe_u32 s34, s34, 0x30007\n"
"s_xor_b32 s30, s30, s34\n"
"s_xor_b32 s30, s30, 0\n"
"s_mov_b32 s31, 1000000\n"
"s_lshl_b32 s38, s30, 7\n"
"s_mul_i32 s39, s20, 344064\n"
"s_add_u32 s39, s39, s38\n"
"s_add_u32 s66, s14, s39\n"
"s_addc_u32 s67, s15, 0\n"
"s_mul_i32 s39, s20, 393216\n"
"s_add_u32 s39, s39, s38\n"
"s_nop 0\n"
"s_add_u32 s39, s39, 32768\n"
"s_add_u32 s68, s14, s39\n"
"s_addc_u32 s69, s15, 0\n"
"s_mov_b32 s70, 196608\n"
"s_mov_b32 s71, 245760\n"
"s_mov_b32 s72, 294912\n"
"s_mov_b32 s73, 344064\n"
"s_cmp_eq_u32 s20, 3\n"
"s_nop 0\n"
"s_cselect_b32 s70, 163840, s70\n"
"s_cselect_b32 s71, 180224, s71\n"
"s_cselect_b32 s72, 0, s72\n"
"s_cselect_b32 s73, 0, s73\n"
"s_mul_i32 s74, s20, 12288\n"
"s_add_u32 s74, s74, 98304\n"
"v_lshrrev_b32 v9, 4, v0\n"
"v_and_b32 v10, 7, v0\n"
"v_lshlrev_b32 v13, 1, v9\n"
"v_xor_b32 v13, v13, v10\n"
"v_lshlrev_b32 v13, 4, v13\n"
"v_lshrrev_b32 v8, 3, v0\n"
"v_lshl_add_u32 v32, v8, 10, v13\n"
"v_add_u32 v33, 0x2000, v32\n"
"v_add_u32 v34, 0x4000, v32\n"
"v_add_u32 v35, 0x6000, v32\n"
"v_lshrrev_b32_e64 v8, 5, v0\n"
"v_lshl_add_u32 v36, v8, 12, v32\n"
"v_add_u32 v37, 0x1000, v36\n"
"v_lshrrev_b32_e64 v8, 2, v0\n"
"v_bfe_u32 v9, v0, 3, 2\n"
"v_and_b32 v10, 3, v0\n"
"v_lshlrev_b32 v8, 7, v8\n"
"v_lshl_add_u32 v8, v9, 5, v8\n"
"v_lshl_add_u32 v8, v10, 3, v8\n"
"v_add_u32 v1, s74, v8\n"
"v_xor_b32 v2, 64, v1\n"
"v_xor_b32 v38, 32, v1\n"
"v_xor_b32 v39, 32, v2\n"
"s_lshl2_add_u32 s38, s20, 161824\n"
"v_mov_b32 v3, s38\n"
"v_lshlrev_b32 v10, 2, v10\n"
"v_add_u32 v4, 161840, v10\n"
"s_lshr_b32 s38, s30, 2\n"
"s_lshl3_add_u32 s38, s29, s38\n"
"s_lshl2_add_u32 s38, s38, s20\n"
"s_lshl_b32 s38, s38, 8\n"
"s_add_u32 s75, s38, 10485760\n"
"s_mul_i32 s38, s29, 43\n"
"s_lshr_b32 s38, s38, 7\n"
"s_mul_i32 s38, s38, 3\n"
"s_sub_u32 s38, s29, s38\n"
"s_lshl_b32 s38, s38, 3\n"
"s_nop 0\n"
"s_lshl_b32 s38, 0xffff, s38\n"
"s_bfe_u32 s38, s38, 0x100008\n"
"s_and_b32 s42, s38, 0x7f7f\n"
"s_and_b32 s44, s38, 0x3f3f\n"
"s_mov_b32 s43, 0\n"
"s_cmp_lt_u32 s20, 3\n"
"s_cselect_b32 s44, s38, s44\n"
"s_mov_b32 s45, 0\n"
"s_waitcnt lgkmcnt(0)\n"
"s_nop 0\n"
"s_add_u32 s32, s32, 0x9e3779b9\n"
"s_addc_u32 s33, s33, 0x7f4a7c15\n"
"v_mov_b32 v28, 161824\n"
"v_mov_b32 v44, s32\n"
"v_mov_b32 v45, s33\n"
"v_mov_b64 v[46:47], 0\n"
"v_mov_b64 v[48:49], 0\n"
"ds_write_b64 v28, v[44:45] offset:32\n"
"ds_write_b128 v28, v[46:49]\n"
"ds_write_b128 v28, v[46:49] offset:16\n"
"ds_write_b128 v28, v[46:49] offset:48\n"
"s_add_u32 s76, s18, 65536\n"
"s_addc_u32 s77, s19, 0\n"
"s_lshl2_add_u32 s34, s24, s25\n"
"s_lshl_b32 s34, s34, 5\n"
"s_add_u32 s34, s34, s26\n"
"s_lshl_b32 s34, s34, 8\n"
"v_lshlrev_b32 v31, 4, v0\n"
"v_add_u32_e64 v30, s34, v31\n"
"v_add_u32 v31, 161888, v31\n"
"s_waitcnt lgkmcnt(0)\n"
"s_barrier\n"
"s_cmp_lg_u32 s20, 0\n"
"s_cbranch_scc1 .Lfb_PS_24\n"
"s_mov_b64 exec, 0xffff\n"
"ds_read_b128 v[40:43], v31\n"
"s_waitcnt lgkmcnt(0)\n"
"v_mov_b32 v43, s32\n"
"global_store_dwordx4 v30, v[40:43], s[76:77] sc1\n"
"s_mov_b64 exec, -1\n"
".Lfb_PS_24:\n"
"s_add_u32 s58, s66, 0\n"
"s_addc_u32 s59, s67, 0\n"
"s_add_u32 m0, s74, 0\n"
"s_nop 0\n"
"global_load_lds_dwordx4 v32, s[58:59]\n"
"s_add_u32 m0, s74, 1024\n"
"s_nop 0\n"
"global_load_lds_dwordx4 v33, s[58:59]\n"
"s_add_u32 m0, s74, 2048\n"
"s_nop 0\n"
"global_load_lds_dwordx4 v34, s[58:59]\n"
"s_add_u32 m0, s74, 3072\n"
"s_nop 0\n"
"global_load_lds_dwordx4 v35, s[58:59]\n"
"s_add_u32 s58, s66, 49152\n"
"s_addc_u32 s59, s67, 0\n"
"s_add_u32 m0, s74, 4096\n"
"s_nop 0\n"
"global_load_lds_dwordx4 v32, s[58:59]\n"
"s_add_u32 m0, s74, 5120\n"
"s_nop 0\n"
"global_load_lds_dwordx4 v33, s[58:59]\n"
"s_add_u32 m0, s74, 6144\n"
"s_nop 0\n"
"global_load_lds_dwordx4 v34, s[58:59]\n"
"s_add_u32 m0, s74, 7168\n"
"s_nop 0\n"
"global_load_lds_dwordx4 v35, s[58:59]\n"
"s_add_u32 s58, s66, 98304\n"
"s_addc_u32 s59, s67, 0\n"
"s_add_u32 m0, s74, 8192\n"
"s_nop 0\n"
"global_load_lds_dwordx4 v32, s[58:59]\n"
"s_add_u32 m0, s74, 9216\n"
"s_nop 0\n"
"global_load_lds_dwordx4 v33, s[58:59]\n"
"s_branch .Lfb_H_P2\n"
".Lfb_C_START:\n"
"s_and_b32 s34, s26, 3\n"
"s_and_b32 s29, s2, 7\n"
"s_lshl2_add_u32 s29, s29, s34\n"
"s_lshr_b32 s30, s26, 2\n"
"s_sub_u32 s34, 0x1ff, s14\n"
"s_bfe_u32 s34, s34, 0x30007\n"
"s_xor_b32 s30, s30, s34\n"
"s_xor_b32 s30, s30, 0\n"
"s_mov_b32 s31, 1000000\n"
"s_lshr_b32 s46, s20, 1\n"
"s_and_b32 s47, s20, 1\n"
"s_lshl_b32 s36, s47, 1\n"
"s_cmp_lt_u32 s22, 2\n"
"s_cselect_b32 s46, 0, s46\n"
"s_cselect_b32 s36, s20, s36\n"
"v_mov_b64 v[16:17], 0\n"
"v_mov_b64 v[18:19], 0\n"
"v_lshlrev_b32 v5, 4, v0\n"
"v_and_b32 v20, 15, v0\n"
"v_lshrrev_b32 v21, 4, v0\n"
"v_bfe_u32 v22, v0, 2, 2\n"
"v_and_b32 v23, 3, v0\n"
"v_and_b32 v24, 1, v21\n"
"v_lshl_add_u32 v25, v21, 3, v22\n"
"s_mul_i32 s34, s46, 96\n"
"v_add_u32 v8, s34, v20\n"
"v_lshrrev_b32 v26, 1, v20\n"
"v_xor_b32 v26, v26, v21\n"
"v_lshlrev_b32 v26, 4, v26\n"
"v_lshl_add_u32 v1, v8, 7, v26\n"
"v_xor_b32 v2, 64, v1\n"
"v_lshlrev_b32 v7, 5, v8\n"
"v_add_u32 v7, 155680, v7\n"
"s_lshl_b32 s34, s36, 8\n"
"s_add_u32 s34, s34, 147456\n"
"v_lshl_add_u32 v6, v20, 4, s34\n"
"v_mul_u32_u24 v26, 2056, v21\n"
"v_add_u32 v6, v26, v6\n"
"v_mov_b32 v9, 161824\n"
"s_mul_i32 s34, s46, 3072\n"
"s_add_u32 s34, s34, 155680\n"
"v_lshl_add_u32 v8, v21, 7, s34\n"
"s_lshl1_add_u32 s34, s21, s46\n"
"s_cmp_eq_u32 s34, 21\n"
"s_cselect_b32 s51, 1, 3\n"
"s_mul_i32 s34, s47, 3\n"
"s_add_u32 s35, s34, 1\n"
"s_nop 0\n"
"v_mul_u32_u24 v26, 192, v25\n"
"v_lshl_add_u32 v26, v23, 3, v26\n"
"v_xor_b32 v27, s34, v24\n"
"v_xor_b32 v28, s35, v24\n"
"v_lshl_add_u32 v27, v27, 5, v26\n"
"v_lshl_add_u32 v28, v28, 5, v26\n"
"v_subrev_u32 v28, 32, v28\n"
"v_lshrrev_b32 v22, 1, v22\n"
"v_lshl_or_b32 v22, v24, 1, v22\n"
"v_xor_b32 v22, s20, v22\n"
"v_lshlrev_b32 v26, 7, v25\n"
"v_lshl_add_u32 v26, v23, 3, v26\n"
"v_lshl_add_u32 v29, v22, 5, v26\n"
"v_lshrrev_b32 v30, 1, v23\n"
"v_xor_b32 v30, v30, v23\n"
"v_and_b32 v30, 1, v30\n"
"v_mul_u32_u24 v30, 24, v30\n"
"v_xor_b32 v30, v30, v29\n"
"s_cmp_lt_u32 s22, 2\n"
"s_cselect_b64 vcc, -1, 0\n"
"v_cndmask_b32 v27, v27, v29, vcc\n"
"v_cndmask_b32_e64 v28, v28, v30, vcc\n"
"v_add_u32 v3, 24576, v27\n"
"v_add_u32 v4, 24576, v28\n"
"s_mov_b32 s48, 7\n"
"s_mov_b32 s50, 0\n"
"s_waitcnt vmcnt(3)\n"
"s_nop 0\n"
"v_mfma_f32_32x32x16_bf16 v[112:127], v[16:19], v[16:19], 0\n"
"v_mfma_f32_32x32x16_bf16 v[128:143], v[16:19], v[16:19], 0\n"
"v_mfma_f32_32x32x16_bf16 v[144:159], v[16:19], v[16:19], 0\n"
"v_mfma_f32_32x32x16_bf16 v[160:175], v[16:19], v[16:19], 0\n"
"v_mfma_f32_32x32x16_bf16 v[176:191], v[16:19], v[16:19], 0\n"
"v_mfma_f32_32x32x16_bf16 v[192:207], v[16:19], v[16:19], 0\n"
"v_mfma_f32_32x32x16_bf16 v[208:223], v[16:19], v[16:19], 0\n"
"v_mfma_f32_32x32x16_bf16 v[224:239], v[16:19], v[16:19], 0\n"
"v_mfma_f32_32x32x16_bf16 v[240:255], v[16:19], v[16:19], 0\n"
"v_lshl_add_u32 v61, s20, 1, v61\n"
"v_lshlrev_b32_e64 v61, 5, v61\n"
"v_add_u32 v61, 161888, v61\n"
"s_waitcnt vmcnt(1)\n"
"v_lshlrev_b32 v40, 16, v32\n"
"v_and_b32 v41, 0xffff0000, v32\n"
"v_lshlrev_b32_e64 v42, 16, v33\n"
"v_and_b32 v43, 0xffff0000, v33\n"
"v_lshlrev_b32_e64 v44, 16, v34\n"
"v_and_b32 v45, 0xffff0000, v34\n"
"v_lshlrev_b32_e64 v46, 16, v35\n"
"v_and_b32 v47, 0xffff0000, v35\n"
"v_lshlrev_b32_e64 v48, 16, v36\n"
"v_and_b32 v49, 0xffff0000, v36\n"
"v_lshlrev_b32_e64 v50, 16, v37\n"
"v_and_b32 v51, 0xffff0000, v37\n"
"v_lshlrev_b32_e64 v52, 16, v38\n"
"v_and_b32 v53, 0xffff0000, v38\n"
"v_lshlrev_b32_e64 v54, 16, v39\n"
"v_and_b32 v55, 0xffff0000, v39\n"
"v_pk_add_f32 v[56:57], v[40:41], v[42:43]\n"
"v_pk_add_f32 v[56:57], v[56:57], v[44:45]\n"
"v_pk_add_f32 v[56:57], v[56:57], v[46:47]\n"
"v_pk_add_f32 v[56:57], v[56:57], v[48:49]\n"
"v_pk_add_f32 v[56:57], v[56:57], v[50:51]\n"
"v_pk_add_f32 v[56:57], v[56:57], v[52:53]\n"
"v_pk_add_f32 v[56:57], v[56:57], v[54:55]\n"
"v_add_f32 v56, v56, v57\n"
"s_nop 1\n"
"v_add_f32_dpp v56, v56, v56 quad_perm:[1,0,3,2] row_mask:0xf bank_mask:0xf\n"
"s_nop 0\n"
"s_nop 0\n"
"v_add_f32_dpp v56, v56, v56 quad_perm:[2,3,0,1] row_mask:0xf bank_mask:0xf\n"
"s_nop 0\n"
"s_nop 0\n"
"v_add_f32_dpp v56, v56, v56 row_half_mirror row_mask:0xf bank_mask:0xf\n"
"s_nop 0\n"
"s_nop 0\n"
"v_add_f32_dpp v56, v56, v56 row_mirror row_mask:0xf bank_mask:0xf\n"
"v_mov_b32 v57, v56\n"
"s_nop 1\n"
"v_permlane16_swap_b32 v56, v57\n"
"v_add_f32 v56, v56, v57\n"
"v_mul_f32 v56, 0x3b000000, v56\n"
"v_pk_add_f32 v[40:41], v[40:41], v[56:57] op_sel_hi:[1,0] neg_lo:[0,1] neg_hi:[0,1]\n"
"v_pk_add_f32 v[42:43], v[42:43], v[56:57] op_sel_hi:[1,0] neg_lo:[0,1] neg_hi:[0,1]\n"
"v_pk_add_f32 v[44:45], v[44:45], v[56:57] op_sel_hi:[1,0] neg_lo:[0,1] neg_hi:[0,1]\n"
"v_pk_add_f32 v[46:47], v[46:47], v[56:57] op_sel_hi:[1,0] neg_lo:[0,1] neg_hi:[0,1]\n"
"v_pk_add_f32 v[48:49], v[48:49], v[56:57] op_sel_hi:[1,0] neg_lo:[0,1] neg_hi:[0,1]\n"
"v_pk_add_f32 v[50:51], v[50:51], v[56:57] op_sel_hi:[1,0] neg_lo:[0,1] neg_hi:[0,1]\n"
"v_pk_add_f32 v[52:53], v[52:53], v[56:57] op_sel_hi:[1,0] neg_lo:[0,1] neg_hi:[0,1]\n"
"v_pk_add_f32 v[54:55], v[54:55], v[56:57] op_sel_hi:[1,0] neg_lo:[0,1] neg_hi:[0,1]\n"
"v_pk_mul_f32 v[58:59], v[40:41], v[40:41]\n"
"v_pk_fma_f32 v[58:59], v[42:43], v[42:43], v[58:59]\n"
"v_pk_fma_f32 v[58:59], v[44:45], v[44:45], v[58:59]\n"
"v_pk_fma_f32 v[58:59], v[46:47], v[46:47], v[58:59]\n"
"v_pk_fma_f32 v[58:59], v[48:49], v[48:49], v[58:59]\n"
"v_pk_fma_f32 v[58:59], v[50:51], v[50:51], v[58:59]\n"
"v_pk_fma_f32 v[58:59], v[52:53], v[52:53], v[58:59]\n"
"v_pk_fma_f32 v[58:59], v[54:55], v[54:55], v[58:59]\n"
"v_add_f32 v58, v58, v59\n"
"s_nop 1\n"
"v_add_f32_dpp v58, v58, v58 quad_perm:[1,0,3,2] row_mask:0xf bank_mask:0xf\n"
"s_nop 0\n"
"s_nop 0\n"
"v_add_f32_dpp v58, v58, v58 quad_perm:[2,3,0,1] row_mask:0xf bank_mask:0xf\n"
"s_nop 0\n"
"s_nop 0\n"
"v_add_f32_dpp v58, v58, v58 row_half_mirror row_mask:0xf bank_mask:0xf\n"
"s_nop 0\n"
"s_nop 0\n"
"v_add_f32_dpp v58, v58, v58 row_mirror row_mask:0xf bank_mask:0xf\n"
"v_mov_b32 v59, v58\n"
"s_nop 1\n"
"v_permlane16_swap_b32 v58, v59\n"
"v_add_f32 v58, v58, v59\n"
"v_mul_f32 v58, 0x3b000000, v58\n"
"v_cvt_pk_bf16_f32 v58, v58, v58\n"
"v_lshlrev_b32_e64 v58, 16, v58\n"
"v_add_f32 v58, 0x3727c5ac, v58\n"
"v_cvt_pk_bf16_f32 v58, v58, v58\n"
"v_lshlrev_b32 v58, 16, v58\n"
"v_sqrt_f32 v58, v58\n"
"v_cvt_pk_bf16_f32 v44, -v56, -v56\n"
"v_cvt_pk_bf16_f32 v58, v58, v58\n"
"v_mov_b32 v45, v44\n"
"v_lshlrev_b32 v58, 16, v58\n"
"v_rcp_f32 v46, v58\n"
"v_mul_f32 v42, v58, v58\n"
"v_mul_f32 v41, 0xc0135761, v46\n"
"v_mul_f32_e64 v40, v46, v46\n"
"v_mul_f32 v40, 0x3d372713, v40\n"
"v_mul_f32_e64 v40, v40, v41\n"
"ds_write_b128 v61, v[44:47] offset:16\n"
"ds_write_b128 v61, v[40:43]\n"
"s_waitcnt vmcnt(0) lgkmcnt(0)\n"
"s_cmp_eq_u32 s28, 1\n"
"s_cbranch_scc1 .Lfb_C_IDLE\n"
"s_cmp_lt_u32 s22, 2\n"
"s_cbranch_scc1 .Lfb_C_Q\n"
"s_lshl2_add_u32 s34, s23, s22\n"
"s_sub_u32 s34, s34, 2\n"
"s_lshl1_add_u32 s66, s34, s47\n"
"s_lshl1_add_u32 s36, s21, s46\n"
"s_nop 0\n"
"s_mul_i32 s34, s36, 168\n"
"s_add_u32 s34, s34, s66\n"
"s_lshl_b32 s34, s34, 10\n"
"s_add_u32 s34, s34, 1048576\n"
"s_add_u32 s52, s18, s34\n"
"s_addc_u32 s53, s19, 0\n"
"s_mul_i32 s34, s36, 96\n"
"s_add_u32 s34, s34, s66\n"
"s_lshl_b32 s34, s34, 10\n"
"s_add_u32 s34, s34, 5242880\n"
"s_add_u32 s54, s18, s34\n"
"s_addc_u32 s55, s19, 0\n"
"s_add_u32 s36, s36, s21\n"
"s_lshl_b32 s36, s36, 7\n"
"s_add_u32 s36, s36, s46\n"
"s_lshl_b32 s36, s36, 6\n"
"s_add_u32 s36, s36, 10485760\n"
"s_add_u32 s56, s18, s36\n"
"s_addc_u32 s57, s19, 0\n"
"s_mul_i32 s34, s66, 37\n"
"s_lshr_b32 s34, s34, 8\n"
"s_mul_i32 s34, s34, 200\n"
"s_lshl3_add_u32 s34, s66, s34\n"
"s_lshr_b32 s35, s66, 3\n"
"s_mul_i32 s35, s35, 192\n"
"s_lshl3_add_u32 s35, s66, s35\n"
"s_cmp_eq_u32 s51, 1\n"
"s_cselect_b32 s36, 2, 4\n"
"s_bfm_b64 s[64:65], s36, 0\n"
"s_cmp_ge_u32 s66, 28\n"
"s_cbranch_scc1 .Lfb_C_LS\n"
"s_cmp_eq_u32 s51, 1\n"
"s_cbranch_scc1 .Lfb_C_LPG\n"
"s_barrier\n"
"s_nop 0\n"
"ds_read_b128 v[16:19], v1 offset:0\n"
"ds_read_b64_tr_b16 v[40:41], v3 offset:0\n"
"ds_read_b64_tr_b16 v[42:43], v3 offset:768\n"
"ds_read_b128 v[20:23], v1 offset:2048\n"
"ds_read_b64_tr_b16 v[44:45], v4 offset:32\n"
"ds_read_b64_tr_b16 v[46:47], v4 offset:800\n"
"ds_read_b128 v[24:27], v1 offset:4096\n"
"ds_read_b64_tr_b16 v[48:49], v3 offset:64\n"
"ds_read_b64_tr_b16 v[50:51], v3 offset:832\n"
"ds_read_b128 v[28:31], v1 offset:6144\n"
"ds_read_b64_tr_b16 v[52:53], v3 offset:12288\n"
"ds_read_b64_tr_b16 v[54:55], v3 offset:13056\n"
"ds_read_b128 v[32:35], v1 offset:8192\n"
"ds_read_b64_tr_b16 v[56:57], v4 offset:12320\n"
"ds_read_b64_tr_b16 v[58:59], v4 offset:13088\n"
"ds_read_b128 v[36:39], v1 offset:10240\n"
"ds_read_b64_tr_b16 v[60:61], v3 offset:12352\n"
"ds_read_b64_tr_b16 v[62:63], v3 offset:13120\n"
".Lfb_C_LOOPG:\n"
"s_waitcnt lgkmcnt(0)\n"
"s_nop 0\n"
"v_mfma_f32_16x16x32_bf16 v[112:115], v[40:43], v[16:19], v[112:115]\n"
"ds_read_b128 v[64:67], v2 offset:0\n"
"v_mfma_f32_16x16x32_bf16 v[136:139], v[44:47], v[16:19], v[136:139]\n"
"ds_read_b64_tr_b16 v[88:89], v3 offset:6144\n"
"v_mfma_f32_16x16x32_bf16 v[160:163], v[48:51], v[16:19], v[160:163]\n"
"ds_read_b64_tr_b16 v[90:91], v3 offset:6912\n"
"v_mfma_f32_16x16x32_bf16 v[184:187], v[52:55], v[16:19], v[184:187]\n"
"ds_read_b128 v[68:71], v2 offset:2048\n"
"v_mfma_f32_16x16x32_bf16 v[208:211], v[56:59], v[16:19], v[208:211]\n"
"ds_read_b64_tr_b16 v[92:93], v4 offset:6176\n"
"v_mfma_f32_16x16x32_bf16 v[232:235], v[60:63], v[16:19], v[232:235]\n"
"ds_read_b64_tr_b16 v[94:95], v4 offset:6944\n"
"v_mfma_f32_16x16x32_bf16 v[236:239], v[60:63], v[20:23], v[236:239]\n"
"ds_read_b128 v[72:75], v2 offset:4096\n"
"v_mfma_f32_16x16x32_bf16 v[212:215], v[56:59], v[20:23], v[212:215]\n"
"ds_read_b64_tr_b16 v[96:97], v3 offset:6208\n"
"v_mfma_f32_16x16x32_bf16 v[188:191], v[52:55], v[20:23], v[188:191]\n"
"ds_read_b64_tr_b16 v[98:99], v3 offset:6976\n"
"v_mfma_f32_16x16x32_bf16 v[164:167], v[48:51], v[20:23], v[164:167]\n"
"ds_read_b128 v[76:79], v2 offset:6144\n"
"v_mfma_f32_16x16x32_bf16 v[140:143], v[44:47], v[20:23], v[140:143]\n"
"ds_read_b64_tr_b16 v[100:101], v3 offset:18432\n"
"v_mfma_f32_16x16x32_bf16 v[116:119], v[40:43], v[20:23], v[116:119]\n"
"ds_read_b64_tr_b16 v[102:103], v3 offset:19200\n"
"v_mfma_f32_16x16x32_bf16 v[120:123], v[40:43], v[24:27], v[120:123]\n"
"ds_read_b128 v[80:83], v2 offset:8192\n"
"v_mfma_f32_16x16x32_bf16 v[144:147], v[44:47], v[24:27], v[144:147]\n"
"ds_read_b64_tr_b16 v[104:105], v4 offset:18464\n"
"v_mfma_f32_16x16x32_bf16 v[168:171], v[48:51], v[24:27], v[168:171]\n"
"ds_read_b64_tr_b16 v[106:107], v4 offset:19232\n"
"v_mfma_f32_16x16x32_bf16 v[192:195], v[52:55], v[24:27], v[192:195]\n"
"ds_read_b128 v[84:87], v2 offset:10240\n"
"v_mfma_f32_16x16x32_bf16 v[216:219], v[56:59], v[24:27], v[216:219]\n"
"ds_read_b64_tr_b16 v[108:109], v3 offset:18496\n"
"v_mfma_f32_16x16x32_bf16 v[240:243], v[60:63], v[24:27], v[240:243]\n"
"ds_read_b64_tr_b16 v[110:111], v3 offset:19264\n"
"v_mfma_f32_16x16x32_bf16 v[244:247], v[60:63], v[28:31], v[244:247]\n"
"s_add_u32 s50, s50, 1\n"
"v_mfma_f32_16x16x32_bf16 v[220:223], v[56:59], v[28:31], v[220:223]\n"
"s_cmp_eq_u32 s50, 3\n"
"v_mfma_f32_16x16x32_bf16 v[196:199], v[52:55], v[28:31], v[196:199]\n"
"s_cselect_b32 s50, 0, s50\n"
"s_nop 0\n"
"v_mfma_f32_16x16x32_bf16 v[172:175], v[48:51], v[28:31], v[172:175]\n"
"s_mov_b32 s49, 49152\n"
"v_mfma_f32_16x16x32_bf16 v[148:151], v[44:47], v[28:31], v[148:151]\n"
"s_cselect_b32 s49, 4294868992, s49\n"
"v_mfma_f32_16x16x32_bf16 v[124:127], v[40:43], v[28:31], v[124:127]\n"
"v_add_u32_e64 v1, s49, v1\n"
"v_mfma_f32_16x16x32_bf16 v[128:131], v[40:43], v[32:35], v[128:131]\n"
"v_add_u32_e64 v2, s49, v2\n"
"v_mfma_f32_16x16x32_bf16 v[152:155], v[44:47], v[32:35], v[152:155]\n"
"v_add_u32_e64 v3, s49, v3\n"
"v_mfma_f32_16x16x32_bf16 v[176:179], v[48:51], v[32:35], v[176:179]\n"
"v_add_u32_e64 v4, s49, v4\n"
"v_mfma_f32_16x16x32_bf16 v[200:203], v[52:55], v[32:35], v[200:203]\n"
"v_mfma_f32_16x16x32_bf16 v[224:227], v[56:59], v[32:35], v[224:227]\n"
"v_mfma_f32_16x16x32_bf16 v[248:251], v[60:63], v[32:35], v[248:251]\n"
"v_mfma_f32_16x16x32_bf16 v[252:255], v[60:63], v[36:39], v[252:255]\n"
"v_mfma_f32_16x16x32_bf16 v[228:231], v[56:59], v[36:39], v[228:231]\n"
"v_mfma_f32_16x16x32_bf16 v[204:207], v[52:55], v[36:39], v[204:207]\n"
"v_mfma_f32_16x16x32_bf16 v[180:183], v[48:51], v[36:39], v[180:183]\n"
"v_mfma_f32_16x16x32_bf16 v[156:159], v[44:47], v[36:39], v[156:159]\n"
"v_mfma_f32_16x16x32_bf16 v[132:135], v[40:43], v[36:39], v[132:135]\n"
"s_waitcnt lgkmcnt(0)\n"
"s_nop 0\n"
"v_mfma_f32_16x16x32_bf16 v[112:115], v[88:91], v[64:67], v[112:115]\n"
"v_mfma_f32_16x16x32_bf16 v[136:139], v[92:95], v[64:67], v[136:139]\n"
"v_mfma_f32_16x16x32_bf16 v[160:163], v[96:99], v[64:67], v[160:163]\n"
"v_mfma_f32_16x16x32_bf16 v[184:187], v[100:103], v[64:67], v[184:187]\n"
"v_mfma_f32_16x16x32_bf16 v[208:211], v[104:107], v[64:67], v[208:211]\n"
"v_mfma_f32_16x16x32_bf16 v[232:235], v[108:111], v[64:67], v[232:235]\n"
"v_mfma_f32_16x16x32_bf16 v[236:239], v[108:111], v[68:71], v[236:239]\n"
"v_mfma_f32_16x16x32_bf16 v[212:215], v[104:107], v[68:71], v[212:215]\n"
"v_mfma_f32_16x16x32_bf16 v[188:191], v[100:103], v[68:71], v[188:191]\n"
"s_barrier\n"
"s_nop 0\n"
"ds_read_b128 v[16:19], v1 offset:0\n"
"v_mfma_f32_16x16x32_bf16 v[164:167], v[96:99], v[68:71], v[164:167]\n"
"ds_read_b64_tr_b16 v[40:41], v3 offset:0\n"
"v_mfma_f32_16x16x32_bf16 v[140:143], v[92:95], v[68:71], v[140:143]\n"
"ds_read_b64_tr_b16 v[42:43], v3 offset:768\n"
"v_mfma_f32_16x16x32_bf16 v[116:119], v[88:91], v[68:71], v[116:119]\n"
"ds_read_b128 v[20:23], v1 offset:2048\n"
"v_mfma_f32_16x16x32_bf16 v[120:123], v[88:91], v[72:75], v[120:123]\n"
"ds_read_b64_tr_b16 v[44:45], v4 offset:32\n"
"v_mfma_f32_16x16x32_bf16 v[144:147], v[92:95], v[72:75], v[144:147]\n"
"ds_read_b64_tr_b16 v[46:47], v4 offset:800\n"
"v_mfma_f32_16x16x32_bf16 v[168:171], v[96:99], v[72:75], v[168:171]\n"
"ds_read_b128 v[24:27], v1 offset:4096\n"
"v_mfma_f32_16x16x32_bf16 v[192:195], v[100:103], v[72:75], v[192:195]\n"
"ds_read_b64_tr_b16 v[48:49], v3 offset:64\n"
"v_mfma_f32_16x16x32_bf16 v[216:219], v[104:107], v[72:75], v[216:219]\n"
"ds_read_b64_tr_b16 v[50:51], v3 offset:832\n"
"v_mfma_f32_16x16x32_bf16 v[240:243], v[108:111], v[72:75], v[240:243]\n"
"ds_read_b128 v[28:31], v1 offset:6144\n"
"v_mfma_f32_16x16x32_bf16 v[244:247], v[108:111], v[76:79], v[244:247]\n"
"ds_read_b64_tr_b16 v[52:53], v3 offset:12288\n"
"v_mfma_f32_16x16x32_bf16 v[220:223], v[104:107], v[76:79], v[220:223]\n"
"ds_read_b64_tr_b16 v[54:55], v3 offset:13056\n"
"v_mfma_f32_16x16x32_bf16 v[196:199], v[100:103], v[76:79], v[196:199]\n"
"ds_read_b128 v[32:35], v1 offset:8192\n"
"v_mfma_f32_16x16x32_bf16 v[172:175], v[96:99], v[76:79], v[172:175]\n"
"ds_read_b64_tr_b16 v[56:57], v4 offset:12320\n"
"v_mfma_f32_16x16x32_bf16 v[148:151], v[92:95], v[76:79], v[148:151]\n"
"ds_read_b64_tr_b16 v[58:59], v4 offset:13088\n"
"v_mfma_f32_16x16x32_bf16 v[124:127], v[88:91], v[76:79], v[124:127]\n"
"ds_read_b128 v[36:39], v1 offset:10240\n"
"v_mfma_f32_16x16x32_bf16 v[128:131], v[88:91], v[80:83], v[128:131]\n"
"ds_read_b64_tr_b16 v[60:61], v3 offset:12352\n"
"v_mfma_f32_16x16x32_bf16 v[152:155], v[92:95], v[80:83], v[152:155]\n"
"ds_read_b64_tr_b16 v[62:63], v3 offset:13120\n"
"v_mfma_f32_16x16x32_bf16 v[176:179], v[96:99], v[80:83], v[176:179]\n"
"s_sub_u32 s48, s48, 1\n"
"v_mfma_f32_16x16x32_bf16 v[200:203], v[100:103], v[80:83], v[200:203]\n"
"s_cmp_lg_u32 s48, 0\n"
"v_mfma_f32_16x16x32_bf16 v[224:227], v[104:107], v[80:83], v[224:227]\n"
"v_mfma_f32_16x16x32_bf16 v[248:251], v[108:111], v[80:83], v[248:251]\n"
"v_mfma_f32_16x16x32_bf16 v[252:255], v[108:111], v[84:87], v[252:255]\n"
"v_mfma_f32_16x16x32_bf16 v[228:231], v[104:107], v[84:87], v[228:231]\n"
"v_mfma_f32_16x16x32_bf16 v[204:207], v[100:103], v[84:87], v[204:207]\n"
"v_mfma_f32_16x16x32_bf16 v[180:183], v[96:99], v[84:87], v[180:183]\n"
"v_mfma_f32_16x16x32_bf16 v[156:159], v[92:95], v[84:87], v[156:159]\n"
"v_mfma_f32_16x16x32_bf16 v[132:135], v[88:91], v[84:87], v[132:135]\n"
"s_cbranch_scc1 .Lfb_C_LOOPG\n"
".Lfb_C_LEG:\n"
"s_waitcnt lgkmcnt(0)\n"
"v_mfma_f32_16x16x32_bf16 v[112:115], v[40:43], v[16:19], v[112:115]\n"
"ds_read_b64 v[10:11], v9 offset:32\n"
"v_mfma_f32_16x16x32_bf16 v[136:139], v[44:47], v[16:19], v[136:139]\n"
"ds_read_b128 v[64:67], v2 offset:0\n"
"v_mfma_f32_16x16x32_bf16 v[184:187], v[52:55], v[16:19], v[184:187]\n"
"ds_read_b64_tr_b16 v[88:89], v3 offset:6144\n"
"v_mfma_f32_16x16x32_bf16 v[208:211], v[56:59], v[16:19], v[208:211]\n"
"ds_read_b64_tr_b16 v[90:91], v3 offset:6912\n"
"v_mfma_f32_16x16x32_bf16 v[212:215], v[56:59], v[20:23], v[212:215]\n"
"ds_read_b128 v[68:71], v2 offset:2048\n"
"v_mfma_f32_16x16x32_bf16 v[188:191], v[52:55], v[20:23], v[188:191]\n"
"ds_read_b64_tr_b16 v[92:93], v4 offset:6176\n"
"v_mfma_f32_16x16x32_bf16 v[140:143], v[44:47], v[20:23], v[140:143]\n"
"ds_read_b64_tr_b16 v[94:95], v4 offset:6944\n"
"v_mfma_f32_16x16x32_bf16 v[116:119], v[40:43], v[20:23], v[116:119]\n"
"ds_read_b128 v[72:75], v2 offset:4096\n"
"v_mfma_f32_16x16x32_bf16 v[120:123], v[40:43], v[24:27], v[120:123]\n"
"ds_read_b128 v[76:79], v2 offset:6144\n"
"v_mfma_f32_16x16x32_bf16 v[144:147], v[44:47], v[24:27], v[144:147]\n"
"ds_read_b64_tr_b16 v[100:101], v3 offset:18432\n"
"v_mfma_f32_16x16x32_bf16 v[192:195], v[52:55], v[24:27], v[192:195]\n"
"ds_read_b64_tr_b16 v[102:103], v3 offset:19200\n"
"v_mfma_f32_16x16x32_bf16 v[216:219], v[56:59], v[24:27], v[216:219]\n"
"ds_read_b128 v[80:83], v2 offset:8192\n"
"v_mfma_f32_16x16x32_bf16 v[220:223], v[56:59], v[28:31], v[220:223]\n"
"ds_read_b64_tr_b16 v[104:105], v4 offset:18464\n"
"v_mfma_f32_16x16x32_bf16 v[196:199], v[52:55], v[28:31], v[196:199]\n"
"ds_read_b64_tr_b16 v[106:107], v4 offset:19232\n"
"v_mfma_f32_16x16x32_bf16 v[148:151], v[44:47], v[28:31], v[148:151]\n"
"ds_read_b128 v[84:87], v2 offset:10240\n"
"v_mfma_f32_16x16x32_bf16 v[124:127], v[40:43], v[28:31], v[124:127]\n"
"v_mfma_f32_16x16x32_bf16 v[128:131], v[40:43], v[32:35], v[128:131]\n"
"v_mfma_f32_16x16x32_bf16 v[152:155], v[44:47], v[32:35], v[152:155]\n"
"v_mfma_f32_16x16x32_bf16 v[200:203], v[52:55], v[32:35], v[200:203]\n"
"v_mfma_f32_16x16x32_bf16 v[224:227], v[56:59], v[32:35], v[224:227]\n"
"v_mfma_f32_16x16x32_bf16 v[228:231], v[56:59], v[36:39], v[228:231]\n"
"v_mfma_f32_16x16x32_bf16 v[204:207], v[52:55], v[36:39], v[204:207]\n"
"v_mfma_f32_16x16x32_bf16 v[156:159], v[44:47], v[36:39], v[156:159]\n"
"v_mfma_f32_16x16x32_bf16 v[132:135], v[40:43], v[36:39], v[132:135]\n"
"s_waitcnt lgkmcnt(0)\n"
"s_nop 0\n"
"v_mfma_f32_16x16x32_bf16 v[112:115], v[88:91], v[64:67], v[112:115]\n"
"ds_read_b64 v[16:17], v7 offset:16\n"
"v_mfma_f32_16x16x32_bf16 v[136:139], v[92:95], v[64:67], v[136:139]\n"
"ds_read_b64 v[18:19], v7 offset:528\n"
"v_mfma_f32_16x16x32_bf16 v[184:187], v[100:103], v[64:67], v[184:187]\n"
"ds_read_b64 v[20:21], v7 offset:1040\n"
"v_mfma_f32_16x16x32_bf16 v[208:211], v[104:107], v[64:67], v[208:211]\n"
"ds_read_b64 v[22:23], v7 offset:1552\n"
"v_mfma_f32_16x16x32_bf16 v[212:215], v[104:107], v[68:71], v[212:215]\n"
"ds_read_b64 v[24:25], v7 offset:2064\n"
"v_mfma_f32_16x16x32_bf16 v[188:191], v[100:103], v[68:71], v[188:191]\n"
"ds_read_b64 v[26:27], v7 offset:2576\n"
"v_mfma_f32_16x16x32_bf16 v[140:143], v[92:95], v[68:71], v[140:143]\n"
"ds_read2st64_b32 v[40:41], v6 offset0:0 offset1:4\n"
"v_mfma_f32_16x16x32_bf16 v[116:119], v[88:91], v[68:71], v[116:119]\n"
"ds_read2st64_b32 v[46:47], v6 offset0:1 offset1:5\n"
"v_mfma_f32_16x16x32_bf16 v[120:123], v[88:91], v[72:75], v[120:123]\n"
"v_add_u32_e64 v6, 4, v6\n"
"v_mfma_f32_16x16x32_bf16 v[144:147], v[92:95], v[72:75], v[144:147]\n"
"ds_read2st64_b32 v[42:43], v6 offset0:0 offset1:4\n"
"v_mfma_f32_16x16x32_bf16 v[192:195], v[100:103], v[72:75], v[192:195]\n"
"ds_read2st64_b32 v[48:49], v6 offset0:1 offset1:5\n"
"v_mfma_f32_16x16x32_bf16 v[216:219], v[104:107], v[72:75], v[216:219]\n"
"v_add_u32_e64 v6, 4, v6\n"
"v_mfma_f32_16x16x32_bf16 v[220:223], v[104:107], v[76:79], v[220:223]\n"
"ds_read2st64_b32 v[44:45], v6 offset0:0 offset1:4\n"
"v_mfma_f32_16x16x32_bf16 v[196:199], v[100:103], v[76:79], v[196:199]\n"
"ds_read2st64_b32 v[50:51], v6 offset0:1 offset1:5\n"
"v_mfma_f32_16x16x32_bf16 v[148:151], v[92:95], v[76:79], v[148:151]\n"
"ds_read_b128 v[28:31], v7 offset:0\n"
"v_mfma_f32_16x16x32_bf16 v[124:127], v[88:91], v[76:79], v[124:127]\n"
"ds_read_b128 v[32:35], v7 offset:512\n"
"v_mfma_f32_16x16x32_bf16 v[128:131], v[88:91], v[80:83], v[128:131]\n"
"ds_read_b128 v[36:39], v7 offset:1024\n"
"v_mfma_f32_16x16x32_bf16 v[152:155], v[92:95], v[80:83], v[152:155]\n"
"ds_read_b128 v[52:55], v7 offset:1536\n"
"v_mfma_f32_16x16x32_bf16 v[200:203], v[100:103], v[80:83], v[200:203]\n"
"ds_read_b128 v[56:59], v7 offset:2048\n"
"ds_read_b128 v[60:63], v7 offset:2560\n"
"v_mfma_f32_16x16x32_bf16 v[224:227], v[104:107], v[80:83], v[224:227]\n"
"v_readfirstlane_b32 s32, v10\n"
"v_readfirstlane_b32 s33, v11\n"
"v_mfma_f32_16x16x32_bf16 v[228:231], v[104:107], v[84:87], v[228:231]\n"
"v_mfma_f32_16x16x32_bf16 v[204:207], v[100:103], v[84:87], v[204:207]\n"
"v_mfma_f32_16x16x32_bf16 v[156:159], v[92:95], v[84:87], v[156:159]\n"
"v_mfma_f32_16x16x32_bf16 v[132:135], v[88:91], v[84:87], v[132:135]\n"
"s_waitcnt lgkmcnt(0)\n"
"s_nop 0\n"
"v_mfma_f32_16x16x16_bf16 v[112:115], v[40:41], v[16:17], v[112:115]\n"
"v_add_u32 v65, 0x7000, v5\n"
"v_add_u32 v66, 0xe000, v5\n"
"v_mfma_f32_16x16x16_bf16 v[136:139], v[42:43], v[16:17], v[136:139]\n"
"v_add_u32 v67, 0x15000, v5\n"
"v_add_u32 v68, 0x1c000, v5\n"
"v_mfma_f32_16x16x16_bf16 v[184:187], v[46:47], v[16:17], v[184:187]\n"
"v_add_u32 v69, 0x23000, v5\n"
"v_mfma_f32_16x16x16_bf16 v[208:211], v[48:49], v[16:17], v[208:211]\n"
"s_cmp_ge_u32 s66, 28\n"
"s_cbranch_scc1 .Lfb_EG_B\n"
"v_pk_mul_f32 v[88:89], v[112:113], v[112:113]\n"
"v_pk_mul_f32 v[90:91], v[114:115], v[114:115]\n"
"v_pk_mul_f32 v[92:93], v[136:137], v[136:137]\n"
"v_pk_mul_f32 v[94:95], v[138:139], v[138:139]\n"
"v_pk_fma_f32 v[88:89], v[88:89], v[28:29], v[28:29] op_sel:[0,0,1] op_sel_hi:[1,0,1]\n"
"v_pk_fma_f32 v[90:91], v[90:91], v[28:29], v[28:29] op_sel:[0,0,1] op_sel_hi:[1,0,1]\n"
"v_pk_fma_f32 v[92:93], v[92:93], v[28:29], v[28:29] op_sel:[0,0,1] op_sel_hi:[1,0,1]\n"
"v_pk_fma_f32 v[94:95], v[94:95], v[28:29], v[28:29] op_sel:[0,0,1] op_sel_hi:[1,0,1]\n"
"v_pk_mul_f32 v[88:89], v[88:89], v[112:113]\n"
"v_pk_mul_f32 v[90:91], v[90:91], v[114:115]\n"
"v_pk_mul_f32 v[92:93], v[92:93], v[136:137]\n"
"v_pk_mul_f32 v[94:95], v[94:95], v[138:139]\n"
"v_mfma_f32_16x16x16_bf16 v[116:119], v[40:41], v[18:19], v[116:119]\n"
"v_exp_f32 v88, v88\n"
"v_exp_f32 v89, v89\n"
"v_mfma_f32_16x16x16_bf16 v[140:143], v[42:43], v[18:19], v[140:143]\n"
"v_exp_f32 v90, v90\n"
"v_exp_f32 v91, v91\n"
"v_mfma_f32_16x16x16_bf16 v[188:191], v[46:47], v[18:19], v[188:191]\n"
"v_exp_f32 v92, v92\n"
"v_exp_f32 v93, v93\n"
"v_mfma_f32_16x16x16_bf16 v[212:215], v[48:49], v[18:19], v[212:215]\n"
"v_exp_f32 v94, v94\n"
"v_exp_f32 v95, v95\n"
"v_pk_mul_f32 v[112:113], v[112:113], v[184:185]\n"
"v_pk_mul_f32 v[114:115], v[114:115], v[186:187]\n"
"v_pk_mul_f32 v[136:137], v[136:137], v[208:209]\n"
"v_pk_mul_f32 v[138:139], v[138:139], v[210:211]\n"
"v_pk_fma_f32 v[88:89], v[88:89], v[30:31], v[30:31] op_sel_hi:[1,0,0]\n"
"v_pk_fma_f32 v[90:91], v[90:91], v[30:31], v[30:31] op_sel_hi:[1,0,0]\n"
"v_pk_fma_f32 v[92:93], v[92:93], v[30:31], v[30:31] op_sel_hi:[1,0,0]\n"
"v_pk_fma_f32 v[94:95], v[94:95], v[30:31], v[30:31] op_sel_hi:[1,0,0]\n"
"v_rcp_f32 v88, v88\n"
"v_rcp_f32 v89, v89\n"
"v_rcp_f32 v90, v90\n"
"v_rcp_f32 v91, v91\n"
"v_rcp_f32 v92, v92\n"
"v_rcp_f32 v93, v93\n"
"v_rcp_f32 v94, v94\n"
"v_rcp_f32 v95, v95\n"
"v_pk_mul_f32 v[112:113], v[112:113], v[88:89]\n"
"v_pk_mul_f32 v[114:115], v[114:115], v[90:91]\n"
"v_pk_mul_f32 v[136:137], v[136:137], v[92:93]\n"
"v_pk_mul_f32 v[138:139], v[138:139], v[94:95]\n"
"v_cvt_pk_bf16_f32 v104, v112, v113\n"
"v_cvt_pk_bf16_f32 v106, v136, v137\n"
"v_cvt_pk_bf16_f32 v105, v114, v115\n"
"v_cvt_pk_bf16_f32 v107, v138, v139\n"
"global_store_dwordx4 v5, v[104:107], s[52:53] sc1\n"
"v_pk_mul_f32 v[96:97], v[116:117], v[116:117]\n"
"v_pk_mul_f32 v[98:99], v[118:119], v[118:119]\n"
"v_pk_mul_f32 v[100:101], v[140:141], v[140:141]\n"
"v_pk_mul_f32 v[102:103], v[142:143], v[142:143]\n"
"v_pk_fma_f32 v[96:97], v[96:97], v[32:33], v[32:33] op_sel:[0,0,1] op_sel_hi:[1,0,1]\n"
"v_pk_fma_f32 v[98:99], v[98:99], v[32:33], v[32:33] op_sel:[0,0,1] op_sel_hi:[1,0,1]\n"
"v_pk_fma_f32 v[100:101], v[100:101], v[32:33], v[32:33] op_sel:[0,0,1] op_sel_hi:[1,0,1]\n"
"v_pk_fma_f32 v[102:103], v[102:103], v[32:33], v[32:33] op_sel:[0,0,1] op_sel_hi:[1,0,1]\n"
"v_pk_mul_f32 v[96:97], v[96:97], v[116:117]\n"
"v_pk_mul_f32 v[98:99], v[98:99], v[118:119]\n"
"v_pk_mul_f32 v[100:101], v[100:101], v[140:141]\n"
"v_pk_mul_f32 v[102:103], v[102:103], v[142:143]\n"
"v_mfma_f32_16x16x16_bf16 v[120:123], v[40:41], v[20:21], v[120:123]\n"
"v_exp_f32 v96, v96\n"
"v_exp_f32 v97, v97\n"
"v_mfma_f32_16x16x16_bf16 v[144:147], v[42:43], v[20:21], v[144:147]\n"
"v_exp_f32 v98, v98\n"
"v_exp_f32 v99, v99\n"
"v_mfma_f32_16x16x16_bf16 v[192:195], v[46:47], v[20:21], v[192:195]\n"
"v_exp_f32 v100, v100\n"
"v_exp_f32 v101, v101\n"
"v_mfma_f32_16x16x16_bf16 v[216:219], v[48:49], v[20:21], v[216:219]\n"
"v_exp_f32 v102, v102\n"
"v_exp_f32 v103, v103\n"
"v_pk_mul_f32 v[116:117], v[116:117], v[188:189]\n"
"v_pk_mul_f32 v[118:119], v[118:119], v[190:191]\n"
"v_pk_mul_f32 v[140:141], v[140:141], v[212:213]\n"
"v_pk_mul_f32 v[142:143], v[142:143], v[214:215]\n"
"v_pk_fma_f32 v[96:97], v[96:97], v[34:35], v[34:35] op_sel_hi:[1,0,0]\n"
"v_pk_fma_f32 v[98:99], v[98:99], v[34:35], v[34:35] op_sel_hi:[1,0,0]\n"
"v_pk_fma_f32 v[100:101], v[100:101], v[34:35], v[34:35] op_sel_hi:[1,0,0]\n"
"v_pk_fma_f32 v[102:103], v[102:103], v[34:35], v[34:35] op_sel_hi:[1,0,0]\n"
"v_rcp_f32 v96, v96\n"
"v_rcp_f32 v97, v97\n"
"v_rcp_f32 v98, v98\n"
"v_rcp_f32 v99, v99\n"
"v_rcp_f32 v100, v100\n"
"v_rcp_f32 v101, v101\n"
"v_rcp_f32 v102, v102\n"
"v_rcp_f32 v103, v103\n"
"v_pk_mul_f32 v[116:117], v[116:117], v[96:97]\n"
"v_pk_mul_f32 v[118:119], v[118:119], v[98:99]\n"
"v_pk_mul_f32 v[140:141], v[140:141], v[100:101]\n"
"v_pk_mul_f32 v[142:143], v[142:143], v[102:103]\n"
"v_cvt_pk_bf16_f32 v108, v116, v117\n"
"v_cvt_pk_bf16_f32 v110, v140, v141\n"
"v_cvt_pk_bf16_f32 v109, v118, v119\n"
"v_cvt_pk_bf16_f32 v111, v142, v143\n"
"global_store_dwordx4 v65, v[108:111], s[52:53] sc1\n"
"s_cmp_eq_u32 s51, 1\n"
"s_cbranch_scc1 .Lfb_EG_B\n"
"v_pk_mul_f32 v[88:89], v[120:121], v[120:121]\n"
"v_pk_mul_f32 v[90:91], v[122:123], v[122:123]\n"
"v_pk_mul_f32 v[92:93], v[144:145], v[144:145]\n"
"v_pk_mul_f32 v[94:95], v[146:147], v[146:147]\n"
"v_pk_fma_f32 v[88:89], v[88:89], v[36:37], v[36:37] op_sel:[0,0,1] op_sel_hi:[1,0,1]\n"
"v_pk_fma_f32 v[90:91], v[90:91], v[36:37], v[36:37] op_sel:[0,0,1] op_sel_hi:[1,0,1]\n"
"v_pk_fma_f32 v[92:93], v[92:93], v[36:37], v[36:37] op_sel:[0,0,1] op_sel_hi:[1,0,1]\n"
"v_pk_fma_f32 v[94:95], v[94:95], v[36:37], v[36:37] op_sel:[0,0,1] op_sel_hi:[1,0,1]\n"
"v_pk_mul_f32 v[88:89], v[88:89], v[120:121]\n"
"v_pk_mul_f32 v[90:91], v[90:91], v[122:123]\n"
"v_pk_mul_f32 v[92:93], v[92:93], v[144:145]\n"
"v_pk_mul_f32 v[94:95], v[94:95], v[146:147]\n"
"v_mfma_f32_16x16x16_bf16 v[124:127], v[40:41], v[22:23], v[124:127]\n"
"v_exp_f32 v88, v88\n"
"v_exp_f32 v89, v89\n"
"v_mfma_f32_16x16x16_bf16 v[148:151], v[42:43], v[22:23], v[148:151]\n"
"v_exp_f32 v90, v90\n"
"v_exp_f32 v91, v91\n"
"v_mfma_f32_16x16x16_bf16 v[196:199], v[46:47], v[22:23], v[196:199]\n"
"v_exp_f32 v92, v92\n"
"v_exp_f32 v93, v93\n"
"v_mfma_f32_16x16x16_bf16 v[220:223], v[48:49], v[22:23], v[220:223]\n"
"v_exp_f32 v94, v94\n"
"v_exp_f32 v95, v95\n"
"v_pk_mul_f32 v[120:121], v[120:121], v[192:193]\n"
"v_pk_mul_f32 v[122:123], v[122:123], v[194:195]\n"
"v_pk_mul_f32 v[144:145], v[144:145], v[216:217]\n"
"v_pk_mul_f32 v[146:147], v[146:147], v[218:219]\n"
"v_pk_fma_f32 v[88:89], v[88:89], v[38:39], v[38:39] op_sel_hi:[1,0,0]\n"
"v_pk_fma_f32 v[90:91], v[90:91], v[38:39], v[38:39] op_sel_hi:[1,0,0]\n"
"v_pk_fma_f32 v[92:93], v[92:93], v[38:39], v[38:39] op_sel_hi:[1,0,0]\n"
"v_pk_fma_f32 v[94:95], v[94:95], v[38:39], v[38:39] op_sel_hi:[1,0,0]\n"
"v_rcp_f32 v88, v88\n"
"v_rcp_f32 v89, v89\n"
"v_rcp_f32 v90, v90\n"
"v_rcp_f32 v91, v91\n"
"v_rcp_f32 v92, v92\n"
"v_rcp_f32 v93, v93\n"
"v_rcp_f32 v94, v94\n"
"v_rcp_f32 v95, v95\n"
"v_pk_mul_f32 v[120:121], v[120:121], v[88:89]\n"
"v_pk_mul_f32 v[122:123], v[122:123], v[90:91]\n"
"v_pk_mul_f32 v[144:145], v[144:145], v[92:93]\n"
"v_pk_mul_f32 v[146:147], v[146:147], v[94:95]\n"
"v_cvt_pk_bf16_f32 v104, v120, v121\n"
"v_cvt_pk_bf16_f32 v106, v144, v145\n"
"v_cvt_pk_bf16_f32 v105, v122, v123\n"
"v_cvt_pk_bf16_f32 v107, v146, v147\n"
"global_store_dwordx4 v66, v[104:107], s[52:53] sc1\n"
"v_pk_mul_f32 v[96:97], v[124:125], v[124:125]\n"
"v_pk_mul_f32 v[98:99], v[126:127], v[126:127]\n"
"v_pk_mul_f32 v[100:101], v[148:149], v[148:149]\n"
"v_pk_mul_f32 v[102:103], v[150:151], v[150:151]\n"
"v_pk_fma_f32 v[96:97], v[96:97], v[52:53], v[52:53] op_sel:[0,0,1] op_sel_hi:[1,0,1]\n"
"v_pk_fma_f32 v[98:99], v[98:99], v[52:53], v[52:53] op_sel:[0,0,1] op_sel_hi:[1,0,1]\n"
"v_pk_fma_f32 v[100:101], v[100:101], v[52:53], v[52:53] op_sel:[0,0,1] op_sel_hi:[1,0,1]\n"
"v_pk_fma_f32 v[102:103], v[102:103], v[52:53], v[52:53] op_sel:[0,0,1] op_sel_hi:[1,0,1]\n"
"v_pk_mul_f32 v[96:97], v[96:97], v[124:125]\n"
"v_pk_mul_f32 v[98:99], v[98:99], v[126:127]\n"
"v_pk_mul_f32 v[100:101], v[100:101], v[148:149]\n"
"v_pk_mul_f32 v[102:103], v[102:103], v[150:151]\n"
"v_mfma_f32_16x16x16_bf16 v[128:131], v[40:41], v[24:25], v[128:131]\n"
"v_exp_f32 v96, v96\n"
"v_exp_f32 v97, v97\n"
"v_mfma_f32_16x16x16_bf16 v[152:155], v[42:43], v[24:25], v[152:155]\n"
"v_exp_f32 v98, v98\n"
"v_exp_f32 v99, v99\n"
"v_mfma_f32_16x16x16_bf16 v[200:203], v[46:47], v[24:25], v[200:203]\n"
"v_exp_f32 v100, v100\n"
"v_exp_f32 v101, v101\n"
"v_mfma_f32_16x16x16_bf16 v[224:227], v[48:49], v[24:25], v[224:227]\n"
"v_exp_f32 v102, v102\n"
"v_exp_f32 v103, v103\n"
"v_pk_mul_f32 v[124:125], v[124:125], v[196:197]\n"
"v_pk_mul_f32 v[126:127], v[126:127], v[198:199]\n"
"v_pk_mul_f32 v[148:149], v[148:149], v[220:221]\n"
"v_pk_mul_f32 v[150:151], v[150:151], v[222:223]\n"
"v_pk_fma_f32 v[96:97], v[96:97], v[54:55], v[54:55] op_sel_hi:[1,0,0]\n"
"v_pk_fma_f32 v[98:99], v[98:99], v[54:55], v[54:55] op_sel_hi:[1,0,0]\n"
"v_pk_fma_f32 v[100:101], v[100:101], v[54:55], v[54:55] op_sel_hi:[1,0,0]\n"
"v_pk_fma_f32 v[102:103], v[102:103], v[54:55], v[54:55] op_sel_hi:[1,0,0]\n"
"v_rcp_f32 v96, v96\n"
"v_rcp_f32 v97, v97\n"
"v_rcp_f32 v98, v98\n"
"v_rcp_f32 v99, v99\n"
"v_rcp_f32 v100, v100\n"
"v_rcp_f32 v101, v101\n"
"v_rcp_f32 v102, v102\n"
"v_rcp_f32 v103, v103\n"
"v_pk_mul_f32 v[124:125], v[124:125], v[96:97]\n"
"v_pk_mul_f32 v[126:127], v[126:127], v[98:99]\n"
"v_pk_mul_f32 v[148:149], v[148:149], v[100:101]\n"
"v_pk_mul_f32 v[150:151], v[150:151], v[102:103]\n"
"v_cvt_pk_bf16_f32 v108, v124, v125\n"
"v_cvt_pk_bf16_f32 v110, v148, v149\n"
"v_cvt_pk_bf16_f32 v109, v126, v127\n"
"v_cvt_pk_bf16_f32 v111, v150, v151\n"
"global_store_dwordx4 v67, v[108:111], s[52:53] sc1\n"
"v_pk_mul_f32 v[88:89], v[128:129], v[128:129]\n"
"v_pk_mul_f32 v[90:91], v[130:131], v[130:131]\n"
"v_pk_mul_f32 v[92:93], v[152:153], v[152:153]\n"
"v_pk_mul_f32 v[94:95], v[154:155], v[154:155]\n"
"v_pk_fma_f32 v[88:89], v[88:89], v[56:57], v[56:57] op_sel:[0,0,1] op_sel_hi:[1,0,1]\n"
"v_pk_fma_f32 v[90:91], v[90:91], v[56:57], v[56:57] op_sel:[0,0,1] op_sel_hi:[1,0,1]\n"
"v_pk_fma_f32 v[92:93], v[92:93], v[56:57], v[56:57] op_sel:[0,0,1] op_sel_hi:[1,0,1]\n"
"v_pk_fma_f32 v[94:95], v[94:95], v[56:57], v[56:57] op_sel:[0,0,1] op_sel_hi:[1,0,1]\n"
"v_pk_mul_f32 v[88:89], v[88:89], v[128:129]\n"
"v_pk_mul_f32 v[90:91], v[90:91], v[130:131]\n"
"v_pk_mul_f32 v[92:93], v[92:93], v[152:153]\n"
"v_pk_mul_f32 v[94:95], v[94:95], v[154:155]\n"
"v_mfma_f32_16x16x16_bf16 v[132:135], v[40:41], v[26:27], v[132:135]\n"
"v_exp_f32 v88, v88\n"
"v_exp_f32 v89, v89\n"
"v_mfma_f32_16x16x16_bf16 v[156:159], v[42:43], v[26:27], v[156:159]\n"
"v_exp_f32 v90, v90\n"
"v_exp_f32 v91, v91\n"
"v_mfma_f32_16x16x16_bf16 v[204:207], v[46:47], v[26:27], v[204:207]\n"
"v_exp_f32 v92, v92\n"
"v_exp_f32 v93, v93\n"
"v_mfma_f32_16x16x16_bf16 v[228:231], v[48:49], v[26:27], v[228:231]\n"
"v_exp_f32 v94, v94\n"
"v_exp_f32 v95, v95\n"
"v_pk_mul_f32 v[128:129], v[128:129], v[200:201]\n"
"v_pk_mul_f32 v[130:131], v[130:131], v[202:203]\n"
"v_pk_mul_f32 v[152:153], v[152:153], v[224:225]\n"
"v_pk_mul_f32 v[154:155], v[154:155], v[226:227]\n"
"v_pk_fma_f32 v[88:89], v[88:89], v[58:59], v[58:59] op_sel_hi:[1,0,0]\n"
"v_pk_fma_f32 v[90:91], v[90:91], v[58:59], v[58:59] op_sel_hi:[1,0,0]\n"
"v_pk_fma_f32 v[92:93], v[92:93], v[58:59], v[58:59] op_sel_hi:[1,0,0]\n"
"v_pk_fma_f32 v[94:95], v[94:95], v[58:59], v[58:59] op_sel_hi:[1,0,0]\n"
"v_rcp_f32 v88, v88\n"
"v_rcp_f32 v89, v89\n"
"v_rcp_f32 v90, v90\n"
"v_rcp_f32 v91, v91\n"
"v_rcp_f32 v92, v92\n"
"v_rcp_f32 v93, v93\n"
"v_rcp_f32 v94, v94\n"
"v_rcp_f32 v95, v95\n"
"v_pk_mul_f32 v[128:129], v[128:129], v[88:89]\n"
"v_pk_mul_f32 v[130:131], v[130:131], v[90:91]\n"
"v_pk_mul_f32 v[152:153], v[152:153], v[92:93]\n"
"v_pk_mul_f32 v[154:155], v[154:155], v[94:95]\n"
"v_cvt_pk_bf16_f32 v104, v128, v129\n"
"v_cvt_pk_bf16_f32 v106, v152, v153\n"
"v_cvt_pk_bf16_f32 v105, v130, v131\n"
"v_cvt_pk_bf16_f32 v107, v154, v155\n"
"global_store_dwordx4 v68, v[104:107], s[52:53] sc1\n"
"v_pk_mul_f32 v[96:97], v[132:133], v[132:133]\n"
"v_pk_mul_f32 v[98:99], v[134:135], v[134:135]\n"
"v_pk_mul_f32 v[100:101], v[156:157], v[156:157]\n"
"v_pk_mul_f32 v[102:103], v[158:159], v[158:159]\n"
"v_pk_fma_f32 v[96:97], v[96:97], v[60:61], v[60:61] op_sel:[0,0,1] op_sel_hi:[1,0,1]\n"
"v_pk_fma_f32 v[98:99], v[98:99], v[60:61], v[60:61] op_sel:[0,0,1] op_sel_hi:[1,0,1]\n"
"v_pk_fma_f32 v[100:101], v[100:101], v[60:61], v[60:61] op_sel:[0,0,1] op_sel_hi:[1,0,1]\n"
"v_pk_fma_f32 v[102:103], v[102:103], v[60:61], v[60:61] op_sel:[0,0,1] op_sel_hi:[1,0,1]\n"
"v_pk_mul_f32 v[96:97], v[96:97], v[132:133]\n"
"v_pk_mul_f32 v[98:99], v[98:99], v[134:135]\n"
"v_pk_mul_f32 v[100:101], v[100:101], v[156:157]\n"
"v_pk_mul_f32 v[102:103], v[102:103], v[158:159]\n"
"v_exp_f32 v96, v96\n"
"v_exp_f32 v97, v97\n"
"v_exp_f32 v98, v98\n"
"v_exp_f32 v99, v99\n"
"v_exp_f32 v100, v100\n"
"v_exp_f32 v101, v101\n"
"v_exp_f32 v102, v102\n"
"v_exp_f32 v103, v103\n"
"v_pk_mul_f32 v[132:133], v[132:133], v[204:205]\n"
"v_pk_mul_f32 v[134:135], v[134:135], v[206:207]\n"
"v_pk_mul_f32 v[156:157], v[156:157], v[228:229]\n"
"v_pk_mul_f32 v[158:159], v[158:159], v[230:231]\n"
"v_pk_fma_f32 v[96:97], v[96:97], v[62:63], v[62:63] op_sel_hi:[1,0,0]\n"
"v_pk_fma_f32 v[98:99], v[98:99], v[62:63], v[62:63] op_sel_hi:[1,0,0]\n"
"v_pk_fma_f32 v[100:101], v[100:101], v[62:63], v[62:63] op_sel_hi:[1,0,0]\n"
"v_pk_fma_f32 v[102:103], v[102:103], v[62:63], v[62:63] op_sel_hi:[1,0,0]\n"
"v_rcp_f32 v96, v96\n"
"v_rcp_f32 v97, v97\n"
"v_rcp_f32 v98, v98\n"
"v_rcp_f32 v99, v99\n"
"v_rcp_f32 v100, v100\n"
"v_rcp_f32 v101, v101\n"
"v_rcp_f32 v102, v102\n"
"v_rcp_f32 v103, v103\n"
"v_pk_mul_f32 v[132:133], v[132:133], v[96:97]\n"
"v_pk_mul_f32 v[134:135], v[134:135], v[98:99]\n"
"v_pk_mul_f32 v[156:157], v[156:157], v[100:101]\n"
"v_pk_mul_f32 v[158:159], v[158:159], v[102:103]\n"
"v_cvt_pk_bf16_f32 v108, v132, v133\n"
"v_cvt_pk_bf16_f32 v110, v156, v157\n"
"v_cvt_pk_bf16_f32 v109, v134, v135\n"
"v_cvt_pk_bf16_f32 v111, v158, v159\n"
"global_store_dwordx4 v69, v[108:111], s[52:53] sc1\n"
".Lfb_EG_B:\n"
"s_nop 0\n"
"s_nop 6\n"
"v_mfma_f32_16x16x16_bf16 v[160:163], v[44:45], v[16:17], v[160:163]\n"
"ds_read_b128 v[112:115], v1 offset:0\n"
"ds_read_b128 v[116:119], v1 offset:2048\n"
"v_mfma_f32_16x16x16_bf16 v[164:167], v[44:45], v[18:19], v[164:167]\n"
"ds_read_b128 v[120:123], v1 offset:4096\n"
"ds_read_b64_tr_b16 v[136:137], v3 offset:64\n"
"v_mfma_f32_16x16x16_bf16 v[232:235], v[50:51], v[16:17], v[232:235]\n"
"ds_read_b64_tr_b16 v[138:139], v3 offset:832\n"
"ds_read_b128 v[124:127], v1 offset:6144\n"
"v_mfma_f32_16x16x16_bf16 v[236:239], v[50:51], v[18:19], v[236:239]\n"
"ds_read_b128 v[128:131], v1 offset:8192\n"
"ds_read_b128 v[132:135], v1 offset:10240\n"
"v_mfma_f32_16x16x16_bf16 v[168:171], v[44:45], v[20:21], v[168:171]\n"
"ds_read_b64_tr_b16 v[140:141], v3 offset:12352\n"
"ds_read_b64_tr_b16 v[142:143], v3 offset:13120\n"
"v_mfma_f32_16x16x16_bf16 v[172:175], v[44:45], v[22:23], v[172:175]\n"
"v_lshrrev_b32 v75, 1, v0\n"
"v_and_b32 v74, 1, v0\n"
"v_mfma_f32_16x16x16_bf16 v[240:243], v[50:51], v[20:21], v[240:243]\n"
"v_lshl_add_u32 v75, v75, 3, v74\n"
"v_lshl_add_u32 v74, v75, 10, s34\n"
"v_mfma_f32_16x16x16_bf16 v[244:247], v[50:51], v[22:23], v[244:247]\n"
"v_lshl_add_u32 v75, v75, 10, s35\n"
"v_lshl_add_u32 v76, s20, 2, v9\n"
"v_mfma_f32_16x16x16_bf16 v[176:179], v[44:45], v[24:25], v[176:179]\n"
"v_mov_b32_e64 v77, 3\n"
"v_add_u32 v71, 0x8000, v5\n"
"v_mfma_f32_16x16x16_bf16 v[180:183], v[44:45], v[26:27], v[180:183]\n"
"v_add_u32 v72, 0x10000, v5\n"
"v_mfma_f32_16x16x16_bf16 v[248:251], v[50:51], v[24:25], v[248:251]\n"
"v_mfma_f32_16x16x16_bf16 v[252:255], v[50:51], v[26:27], v[252:255]\n"
"s_waitcnt lgkmcnt(0)\n"
"s_nop 0\n"
"v_mfma_f32_16x16x32_bf16 v[160:163], v[136:139], v[112:115], v[160:163]\n"
"ds_read_b128 v[184:187], v2 offset:0\n"
"ds_read_b128 v[188:191], v2 offset:2048\n"
"v_mfma_f32_16x16x32_bf16 v[232:235], v[140:143], v[112:115], v[232:235]\n"
"ds_read_b128 v[192:195], v2 offset:4096\n"
"ds_read_b64_tr_b16 v[208:209], v3 offset:6208\n"
"v_mfma_f32_16x16x32_bf16 v[164:167], v[136:139], v[116:119], v[164:167]\n"
"ds_read_b64_tr_b16 v[210:211], v3 offset:6976\n"
"ds_read_b128 v[196:199], v2 offset:6144\n"
"v_mfma_f32_16x16x32_bf16 v[236:239], v[140:143], v[116:119], v[236:239]\n"
"ds_read_b128 v[200:203], v2 offset:8192\n"
"ds_read_b128 v[204:207], v2 offset:10240\n"
"v_mfma_f32_16x16x32_bf16 v[168:171], v[136:139], v[120:123], v[168:171]\n"
"ds_read_b64_tr_b16 v[212:213], v3 offset:18496\n"
"ds_read_b64_tr_b16 v[214:215], v3 offset:19264\n"
"v_mfma_f32_16x16x32_bf16 v[240:243], v[140:143], v[120:123], v[240:243]\n"
"v_mfma_f32_16x16x32_bf16 v[172:175], v[136:139], v[124:127], v[172:175]\n"
"v_mfma_f32_16x16x32_bf16 v[244:247], v[140:143], v[124:127], v[244:247]\n"
"v_mfma_f32_16x16x32_bf16 v[176:179], v[136:139], v[128:131], v[176:179]\n"
"v_mfma_f32_16x16x32_bf16 v[248:251], v[140:143], v[128:131], v[248:251]\n"
"v_mfma_f32_16x16x32_bf16 v[180:183], v[136:139], v[132:135], v[180:183]\n"
"v_mfma_f32_16x16x32_bf16 v[252:255], v[140:143], v[132:135], v[252:255]\n"
"s_waitcnt lgkmcnt(0)\n"
"s_nop 0\n"
"v_mfma_f32_16x16x32_bf16 v[160:163], v[208:211], v[184:187], v[160:163]\n"
"v_mfma_f32_16x16x32_bf16 v[232:235], v[212:215], v[184:187], v[232:235]\n"
"v_mfma_f32_16x16x32_bf16 v[164:167], v[208:211], v[188:191], v[164:167]\n"
"v_mfma_f32_16x16x32_bf16 v[236:239], v[212:215], v[188:191], v[236:239]\n"
"v_mfma_f32_16x16x32_bf16 v[168:171], v[208:211], v[192:195], v[168:171]\n"
"v_mfma_f32_16x16x32_bf16 v[240:243], v[212:215], v[192:195], v[240:243]\n"
"v_mfma_f32_16x16x32_bf16 v[172:175], v[208:211], v[196:199], v[172:175]\n"
"v_mfma_f32_16x16x32_bf16 v[244:247], v[212:215], v[196:199], v[244:247]\n"
"v_mfma_f32_16x16x32_bf16 v[176:179], v[208:211], v[200:203], v[176:179]\n"
"v_mfma_f32_16x16x32_bf16 v[248:251], v[212:215], v[200:203], v[248:251]\n"
"v_mfma_f32_16x16x32_bf16 v[180:183], v[208:211], v[204:207], v[180:183]\n"
"v_mfma_f32_16x16x32_bf16 v[252:255], v[212:215], v[204:207], v[252:255]\n"
"v_pk_mul_f32 v[88:89], v[160:161], v[160:161]\n"
"v_pk_mul_f32 v[90:91], v[162:163], v[162:163]\n"
"v_pk_mul_f32 v[92:93], v[164:165], v[164:165]\n"
"v_pk_mul_f32 v[94:95], v[166:167], v[166:167]\n"
"ds_write_b32 v76, v77 offset:16\n"
"v_pk_fma_f32 v[88:89], v[88:89], v[28:29], v[28:29] op_sel:[0,0,1] op_sel_hi:[1,0,1]\n"
"v_pk_fma_f32 v[90:91], v[90:91], v[28:29], v[28:29] op_sel:[0,0,1] op_sel_hi:[1,0,1]\n"
"v_pk_fma_f32 v[92:93], v[92:93], v[32:33], v[32:33] op_sel:[0,0,1] op_sel_hi:[1,0,1]\n"
"v_pk_fma_f32 v[94:95], v[94:95], v[32:33], v[32:33] op_sel:[0,0,1] op_sel_hi:[1,0,1]\n"
"v_pk_mul_f32 v[88:89], v[88:89], v[160:161]\n"
"v_pk_mul_f32 v[90:91], v[90:91], v[162:163]\n"
"v_pk_mul_f32 v[92:93], v[92:93], v[164:165]\n"
"v_pk_mul_f32 v[94:95], v[94:95], v[166:167]\n"
"v_exp_f32 v88, v88\n"
"v_exp_f32 v89, v89\n"
"v_exp_f32 v90, v90\n"
"v_exp_f32 v91, v91\n"
"v_exp_f32 v92, v92\n"
"v_exp_f32 v93, v93\n"
"v_exp_f32 v94, v94\n"
"v_exp_f32 v95, v95\n"
"v_pk_mul_f32 v[160:161], v[160:161], v[232:233]\n"
"v_pk_mul_f32 v[162:163], v[162:163], v[234:235]\n"
"v_pk_mul_f32 v[164:165], v[164:165], v[236:237]\n"
"v_pk_mul_f32 v[166:167], v[166:167], v[238:239]\n"
"v_pk_fma_f32 v[88:89], v[88:89], v[30:31], v[30:31] op_sel_hi:[1,0,0]\n"
"v_pk_fma_f32 v[90:91], v[90:91], v[30:31], v[30:31] op_sel_hi:[1,0,0]\n"
"v_pk_fma_f32 v[92:93], v[92:93], v[34:35], v[34:35] op_sel_hi:[1,0,0]\n"
"v_pk_fma_f32 v[94:95], v[94:95], v[34:35], v[34:35] op_sel_hi:[1,0,0]\n"
"v_rcp_f32 v88, v88\n"
"v_rcp_f32 v89, v89\n"
"v_rcp_f32 v90, v90\n"
"v_rcp_f32 v91, v91\n"
"v_rcp_f32 v92, v92\n"
"v_rcp_f32 v93, v93\n"
"v_rcp_f32 v94, v94\n"
"v_rcp_f32 v95, v95\n"
"v_pk_mul_f32 v[160:161], v[160:161], v[88:89]\n"
"v_pk_mul_f32 v[162:163], v[162:163], v[90:91]\n"
"v_pk_mul_f32 v[164:165], v[164:165], v[92:93]\n"
"v_pk_mul_f32 v[166:167], v[166:167], v[94:95]\n"
"v_cvt_pk_bf16_f32 v104, v160, v161\n"
"v_cvt_pk_bf16_f32 v106, v164, v165\n"
"v_cvt_pk_bf16_f32 v105, v162, v163\n"
"v_cvt_pk_bf16_f32 v107, v166, v167\n"
"v_permlane16_swap_b32 v104, v106\n"
"s_nop 0\n"
"v_permlane16_swap_b32 v105, v107\n"
"s_cmp_ge_u32 s66, 28\n"
"s_cbranch_scc1 .Lfb_EG_S1\n"
"s_waitcnt vmcnt(0)\n"
"s_mov_b64 exec, s[64:65]\n"
"s_nop 0\n"
"global_atomic_swap_x2 v74, v[10:11], s[56:57] offset:0 sc1\n"
"s_mov_b64 exec, -1\n"
".Lfb_EG_S1:\n"
"s_nop 1\n"
"global_store_dwordx4 v5, v[104:107], s[54:55] sc1\n"
"s_cmp_eq_u32 s51, 1\n"
"s_cbranch_scc1 .Lfb_EG_D\n"
"v_pk_mul_f32 v[96:97], v[168:169], v[168:169]\n"
"v_pk_mul_f32 v[98:99], v[170:171], v[170:171]\n"
"v_pk_mul_f32 v[100:101], v[172:173], v[172:173]\n"
"v_pk_mul_f32 v[102:103], v[174:175], v[174:175]\n"
"v_pk_fma_f32 v[96:97], v[96:97], v[36:37], v[36:37] op_sel:[0,0,1] op_sel_hi:[1,0,1]\n"
"v_pk_fma_f32 v[98:99], v[98:99], v[36:37], v[36:37] op_sel:[0,0,1] op_sel_hi:[1,0,1]\n"
"v_pk_fma_f32 v[100:101], v[100:101], v[52:53], v[52:53] op_sel:[0,0,1] op_sel_hi:[1,0,1]\n"
"v_pk_fma_f32 v[102:103], v[102:103], v[52:53], v[52:53] op_sel:[0,0,1] op_sel_hi:[1,0,1]\n"
"v_pk_mul_f32 v[96:97], v[96:97], v[168:169]\n"
"v_pk_mul_f32 v[98:99], v[98:99], v[170:171]\n"
"v_pk_mul_f32 v[100:101], v[100:101], v[172:173]\n"
"v_pk_mul_f32 v[102:103], v[102:103], v[174:175]\n"
"v_exp_f32 v96, v96\n"
"v_exp_f32 v97, v97\n"
"v_exp_f32 v98, v98\n"
"v_exp_f32 v99, v99\n"
"v_exp_f32 v100, v100\n"
"v_exp_f32 v101, v101\n"
"v_exp_f32 v102, v102\n"
"v_exp_f32 v103, v103\n"
"v_pk_mul_f32 v[168:169], v[168:169], v[240:241]\n"
"v_pk_mul_f32 v[170:171], v[170:171], v[242:243]\n"
"v_pk_mul_f32 v[172:173], v[172:173], v[244:245]\n"
"v_pk_mul_f32 v[174:175], v[174:175], v[246:247]\n"
"v_pk_fma_f32 v[96:97], v[96:97], v[38:39], v[38:39] op_sel_hi:[1,0,0]\n"
"v_pk_fma_f32 v[98:99], v[98:99], v[38:39], v[38:39] op_sel_hi:[1,0,0]\n"
"v_pk_fma_f32 v[100:101], v[100:101], v[54:55], v[54:55] op_sel_hi:[1,0,0]\n"
"v_pk_fma_f32 v[102:103], v[102:103], v[54:55], v[54:55] op_sel_hi:[1,0,0]\n"
"v_rcp_f32 v96, v96\n"
"v_rcp_f32 v97, v97\n"
"v_rcp_f32 v98, v98\n"
"v_rcp_f32 v99, v99\n"
"v_rcp_f32 v100, v100\n"
"v_rcp_f32 v101, v101\n"
"v_rcp_f32 v102, v102\n"
"v_rcp_f32 v103, v103\n"
"v_pk_mul_f32 v[168:169], v[168:169], v[96:97]\n"
"v_pk_mul_f32 v[170:171], v[170:171], v[98:99]\n"
"v_pk_mul_f32 v[172:173], v[172:173], v[100:101]\n"
"v_pk_mul_f32 v[174:175], v[174:175], v[102:103]\n"
"v_cvt_pk_bf16_f32 v108, v168, v169\n"
"v_cvt_pk_bf16_f32 v110, v172, v173\n"
"v_cvt_pk_bf16_f32 v109, v170, v171\n"
"v_cvt_pk_bf16_f32 v111, v174, v175\n"
"v_permlane16_swap_b32 v108, v110\n"
"s_nop 0\n"
"v_permlane16_swap_b32 v109, v111\n"
"s_nop 0\n"
"global_store_dwordx4 v71, v[108:111], s[54:55] sc1\n"
"v_pk_mul_f32 v[88:89], v[176:177], v[176:177]\n"
"v_pk_mul_f32 v[90:91], v[178:179], v[178:179]\n"
"v_pk_mul_f32 v[92:93], v[180:181], v[180:181]\n"
"v_pk_mul_f32 v[94:95], v[182:183], v[182:183]\n"
"v_pk_fma_f32 v[88:89], v[88:89], v[56:57], v[56:57] op_sel:[0,0,1] op_sel_hi:[1,0,1]\n"
"v_pk_fma_f32 v[90:91], v[90:91], v[56:57], v[56:57] op_sel:[0,0,1] op_sel_hi:[1,0,1]\n"
"v_pk_fma_f32 v[92:93], v[92:93], v[60:61], v[60:61] op_sel:[0,0,1] op_sel_hi:[1,0,1]\n"
"v_pk_fma_f32 v[94:95], v[94:95], v[60:61], v[60:61] op_sel:[0,0,1] op_sel_hi:[1,0,1]\n"
"v_pk_mul_f32 v[88:89], v[88:89], v[176:177]\n"
"v_pk_mul_f32 v[90:91], v[90:91], v[178:179]\n"
"v_pk_mul_f32 v[92:93], v[92:93], v[180:181]\n"
"v_pk_mul_f32 v[94:95], v[94:95], v[182:183]\n"
"v_exp_f32 v88, v88\n"
"v_exp_f32 v89, v89\n"
"v_exp_f32 v90, v90\n"
"v_exp_f32 v91, v91\n"
"v_exp_f32 v92, v92\n"
"v_exp_f32 v93, v93\n"
"v_exp_f32 v94, v94\n"
"v_exp_f32 v95, v95\n"
"v_pk_mul_f32 v[176:177], v[176:177], v[248:249]\n"
"v_pk_mul_f32 v[178:179], v[178:179], v[250:251]\n"
"v_pk_mul_f32 v[180:181], v[180:181], v[252:253]\n"
"v_pk_mul_f32 v[182:183], v[182:183], v[254:255]\n"
"v_pk_fma_f32 v[88:89], v[88:89], v[58:59], v[58:59] op_sel_hi:[1,0,0]\n"
"v_pk_fma_f32 v[90:91], v[90:91], v[58:59], v[58:59] op_sel_hi:[1,0,0]\n"
"v_pk_fma_f32 v[92:93], v[92:93], v[62:63], v[62:63] op_sel_hi:[1,0,0]\n"
"v_pk_fma_f32 v[94:95], v[94:95], v[62:63], v[62:63] op_sel_hi:[1,0,0]\n"
"v_rcp_f32 v88, v88\n"
"v_rcp_f32 v89, v89\n"
"v_rcp_f32 v90, v90\n"
"v_rcp_f32 v91, v91\n"
"v_rcp_f32 v92, v92\n"
"v_rcp_f32 v93, v93\n"
"v_rcp_f32 v94, v94\n"
"v_rcp_f32 v95, v95\n"
"v_pk_mul_f32 v[176:177], v[176:177], v[88:89]\n"
"v_pk_mul_f32 v[178:179], v[178:179], v[90:91]\n"
"v_pk_mul_f32 v[180:181], v[180:181], v[92:93]\n"
"v_pk_mul_f32 v[182:183], v[182:183], v[94:95]\n"
"v_cvt_pk_bf16_f32 v104, v176, v177\n"
"v_cvt_pk_bf16_f32 v106, v180, v181\n"
"v_cvt_pk_bf16_f32 v105, v178, v179\n"
"v_cvt_pk_bf16_f32 v107, v182, v183\n"
"v_permlane16_swap_b32 v104, v106\n"
"s_nop 0\n"
"v_permlane16_swap_b32 v105, v107\n"
"s_nop 0\n"
"global_store_dwordx4 v72, v[104:107], s[54:55] sc1\n"
".Lfb_EG_D:\n"
"s_waitcnt vmcnt(0)\n"
"s_mov_b64 exec, s[64:65]\n"
"global_atomic_swap_x2 v75, v[10:11], s[56:57] offset:128 sc1\n"
"s_mov_b64 exec, -1\n"
"s_branch .Lfb_C_P2\n"
".Lfb_C_Q:\n"
"s_mov_b32 s60, 0x358637bd\n"
"s_mov_b32 s62, 0x3fb8aa3b\n"
"s_mov_b32 s63, 0xb5c1a2a0\n"
"s_lshl1_add_u32 s34, s23, s22\n"
"s_lshl2_add_u32 s66, s34, s20\n"
"s_lshl_b32 s34, s66, 14\n"
"s_add_u32 s34, s34, 262144\n"
"s_add_u32 s52, s18, s34\n"
"s_addc_u32 s53, s19, 0\n"
"s_lshl_b32 s34, s66, 11\n"
"s_add_u32 s34, s34, 9437184\n"
"s_add_u32 s56, s18, s34\n"
"s_addc_u32 s57, s19, 0\n"
"s_add_u32 s54, s56, 65536\n"
"s_addc_u32 s55, s57, 0\n"
"s_mul_i32 s34, s21, 192\n"
"s_add_u32 s34, s34, s66\n"
"s_lshl_b32 s34, s34, 10\n"
"s_add_u32 s34, s34, 7340032\n"
"s_add_u32 s68, s18, s34\n"
"s_addc_u32 s69, s19, 0\n"
"s_mul_i32 s34, s21, 6144\n"
"s_lshl3_add_u32 s34, s66, s34\n"
"s_add_u32 s34, s34, 9961472\n"
"s_add_u32 s74, s18, s34\n"
"s_addc_u32 s75, s19, 0\n"
"s_cmp_eq_u32 s21, 10\n"
"s_cselect_b32 s34, 16, 24\n"
"s_bfm_b64 s[64:65], s34, 0\n"
"s_nop 0\n"
"s_mov_b32 s70, 0x07060500\n"
"s_mov_b32 s71, 0x07060501\n"
"s_mov_b32 s72, 0x07060502\n"
"s_mov_b32 s73, 0x07060503\n"
"s_lshl_b32 s34, s21, 10\n"
"s_lshl_b32 s35, s21, 7\n"
"s_lshl_b32 s36, s21, 3\n"
"s_cmp_eq_u32 s21, 10\n"
"s_cbranch_scc1 .Lfb_C_LPQ\n"
"s_barrier\n"
"ds_read_b64_tr_b16 v[64:65], v3 offset:0\n"
"ds_read_b64_tr_b16 v[66:67], v3 offset:512\n"
"ds_read_b64_tr_b16 v[68:69], v3 offset:4096\n"
"ds_read_b64_tr_b16 v[70:71], v3 offset:4608\n"
"ds_read_b64_tr_b16 v[72:73], v4 offset:8192\n"
"ds_read_b64_tr_b16 v[74:75], v4 offset:8704\n"
"ds_read_b128 v[16:19], v1 offset:0\n"
"ds_read_b128 v[20:23], v1 offset:2048\n"
"ds_read_b128 v[24:27], v1 offset:4096\n"
"ds_read_b128 v[28:31], v1 offset:6144\n"
"ds_read_b128 v[32:35], v1 offset:8192\n"
"ds_read_b128 v[36:39], v1 offset:10240\n"
"ds_read_b128 v[40:43], v1 offset:12288\n"
"ds_read_b128 v[44:47], v1 offset:14336\n"
"ds_read_b128 v[48:51], v1 offset:16384\n"
"ds_read_b128 v[52:55], v1 offset:18432\n"
"ds_read_b128 v[56:59], v1 offset:20480\n"
".Lfb_C_LOOPQ:\n"
"s_waitcnt lgkmcnt(8)\n"
"v_mfma_f32_16x16x32_bf16 v[160:163], v[16:19], v[68:71], v[160:163]\n"
"ds_read_b128 v[60:63], v1 offset:22528\n"
"v_mfma_f32_16x16x32_bf16 v[208:211], v[16:19], v[72:75], v[208:211]\n"
"s_add_u32 s50, s50, 1\n"
"v_mfma_f32_16x16x32_bf16 v[112:115], v[64:67], v[16:19], v[112:115]\n"
"v_mfma_f32_16x16x32_bf16 v[164:167], v[20:23], v[68:71], v[164:167]\n"
"ds_read_b128 v[16:19], v2 offset:0\n"
"v_mfma_f32_16x16x32_bf16 v[212:215], v[20:23], v[72:75], v[212:215]\n"
"ds_read_b64_tr_b16 v[76:77], v3 offset:12288\n"
"v_mfma_f32_16x16x32_bf16 v[116:119], v[64:67], v[20:23], v[116:119]\n"
"s_cmp_eq_u32 s50, 3\n"
"s_nop 0\n"
"v_mfma_f32_16x16x32_bf16 v[168:171], v[24:27], v[68:71], v[168:171]\n"
"ds_read_b128 v[20:23], v2 offset:2048\n"
"v_mfma_f32_16x16x32_bf16 v[216:219], v[24:27], v[72:75], v[216:219]\n"
"ds_read_b64_tr_b16 v[78:79], v3 offset:12800\n"
"v_mfma_f32_16x16x32_bf16 v[120:123], v[64:67], v[24:27], v[120:123]\n"
"s_cselect_b32 s50, 0, s50\n"
"s_waitcnt lgkmcnt(10)\n"
"v_mfma_f32_16x16x32_bf16 v[172:175], v[28:31], v[68:71], v[172:175]\n"
"ds_read_b128 v[24:27], v2 offset:4096\n"
"v_mfma_f32_16x16x32_bf16 v[220:223], v[28:31], v[72:75], v[220:223]\n"
"ds_read_b64_tr_b16 v[80:81], v3 offset:16384\n"
"v_mfma_f32_16x16x32_bf16 v[124:127], v[64:67], v[28:31], v[124:127]\n"
"s_mov_b32 s49, 49152\n"
"v_mfma_f32_16x16x32_bf16 v[176:179], v[32:35], v[68:71], v[176:179]\n"
"ds_read_b128 v[28:31], v2 offset:6144\n"
"v_mfma_f32_16x16x32_bf16 v[224:227], v[32:35], v[72:75], v[224:227]\n"
"ds_read_b64_tr_b16 v[82:83], v3 offset:16896\n"
"v_mfma_f32_16x16x32_bf16 v[128:131], v[64:67], v[32:35], v[128:131]\n"
"s_cselect_b32 s49, 4294868992, s49\n"
"v_mfma_f32_16x16x32_bf16 v[180:183], v[36:39], v[68:71], v[180:183]\n"
"ds_read_b128 v[32:35], v2 offset:8192\n"
"v_mfma_f32_16x16x32_bf16 v[228:231], v[36:39], v[72:75], v[228:231]\n"
"ds_read_b64_tr_b16 v[84:85], v4 offset:20480\n"
"v_mfma_f32_16x16x32_bf16 v[132:135], v[64:67], v[36:39], v[132:135]\n"
"s_waitcnt lgkmcnt(13)\n"
"s_nop 0\n"
"v_mfma_f32_16x16x32_bf16 v[184:187], v[40:43], v[68:71], v[184:187]\n"
"ds_read_b128 v[36:39], v2 offset:10240\n"
"v_mfma_f32_16x16x32_bf16 v[232:235], v[40:43], v[72:75], v[232:235]\n"
"ds_read_b64_tr_b16 v[86:87], v4 offset:20992\n"
"v_mfma_f32_16x16x32_bf16 v[136:139], v[64:67], v[40:43], v[136:139]\n"
"v_mfma_f32_16x16x32_bf16 v[188:191], v[44:47], v[68:71], v[188:191]\n"
"ds_read_b128 v[40:43], v2 offset:12288\n"
"v_mfma_f32_16x16x32_bf16 v[236:239], v[44:47], v[72:75], v[236:239]\n"
"v_add_u32_e64 v1, s49, v1\n"
"v_mfma_f32_16x16x32_bf16 v[140:143], v[64:67], v[44:47], v[140:143]\n"
"v_mfma_f32_16x16x32_bf16 v[192:195], v[48:51], v[68:71], v[192:195]\n"
"ds_read_b128 v[44:47], v2 offset:14336\n"
"v_mfma_f32_16x16x32_bf16 v[240:243], v[48:51], v[72:75], v[240:243]\n"
"v_add_u32_e64 v3, s49, v3\n"
"v_mfma_f32_16x16x32_bf16 v[144:147], v[64:67], v[48:51], v[144:147]\n"
"s_waitcnt lgkmcnt(14)\n"
"v_mfma_f32_16x16x32_bf16 v[196:199], v[52:55], v[68:71], v[196:199]\n"
"ds_read_b128 v[48:51], v2 offset:16384\n"
"v_mfma_f32_16x16x32_bf16 v[244:247], v[52:55], v[72:75], v[244:247]\n"
"v_add_u32 v4, s49, v4\n"
"v_mfma_f32_16x16x32_bf16 v[148:151], v[64:67], v[52:55], v[148:151]\n"
"v_mfma_f32_16x16x32_bf16 v[200:203], v[56:59], v[68:71], v[200:203]\n"
"ds_read_b128 v[52:55], v2 offset:18432\n"
"v_mfma_f32_16x16x32_bf16 v[248:251], v[56:59], v[72:75], v[248:251]\n"
"v_mfma_f32_16x16x32_bf16 v[152:155], v[64:67], v[56:59], v[152:155]\n"
"v_mfma_f32_16x16x32_bf16 v[204:207], v[60:63], v[68:71], v[204:207]\n"
"ds_read_b128 v[56:59], v2 offset:20480\n"
"v_mfma_f32_16x16x32_bf16 v[252:255], v[60:63], v[72:75], v[252:255]\n"
"v_mfma_f32_16x16x32_bf16 v[156:159], v[64:67], v[60:63], v[156:159]\n"
"s_waitcnt lgkmcnt(5)\n"
"s_nop 0\n"
"v_mfma_f32_16x16x32_bf16 v[160:163], v[16:19], v[80:83], v[160:163]\n"
"ds_read_b128 v[60:63], v2 offset:22528\n"
"v_mfma_f32_16x16x32_bf16 v[208:211], v[16:19], v[84:87], v[208:211]\n"
"v_mfma_f32_16x16x32_bf16 v[112:115], v[76:79], v[16:19], v[112:115]\n"
"v_mfma_f32_16x16x32_bf16 v[164:167], v[20:23], v[80:83], v[164:167]\n"
"v_add_u32_e64 v2, s49, v2\n"
"v_mfma_f32_16x16x32_bf16 v[212:215], v[20:23], v[84:87], v[212:215]\n"
"s_sub_u32 s48, s48, 1\n"
"v_mfma_f32_16x16x32_bf16 v[116:119], v[76:79], v[20:23], v[116:119]\n"
"s_cmp_lg_u32 s48, 0\n"
"v_mfma_f32_16x16x32_bf16 v[168:171], v[24:27], v[80:83], v[168:171]\n"
"s_nop 0\n"
"v_mfma_f32_16x16x32_bf16 v[216:219], v[24:27], v[84:87], v[216:219]\n"
"s_waitcnt lgkmcnt(0)\n"
"v_mfma_f32_16x16x32_bf16 v[120:123], v[76:79], v[24:27], v[120:123]\n"
"s_barrier\n"
"s_nop 0\n"
"v_mfma_f32_16x16x32_bf16 v[172:175], v[28:31], v[80:83], v[172:175]\n"
"ds_read_b128 v[16:19], v1 offset:0\n"
"v_mfma_f32_16x16x32_bf16 v[220:223], v[28:31], v[84:87], v[220:223]\n"
"ds_read_b64_tr_b16 v[64:65], v3 offset:0\n"
"v_mfma_f32_16x16x32_bf16 v[124:127], v[76:79], v[28:31], v[124:127]\n"
"v_mfma_f32_16x16x32_bf16 v[176:179], v[32:35], v[80:83], v[176:179]\n"
"ds_read_b64_tr_b16 v[66:67], v3 offset:512\n"
"v_mfma_f32_16x16x32_bf16 v[224:227], v[32:35], v[84:87], v[224:227]\n"
"ds_read_b64_tr_b16 v[68:69], v3 offset:4096\n"
"v_mfma_f32_16x16x32_bf16 v[128:131], v[76:79], v[32:35], v[128:131]\n"
"v_mfma_f32_16x16x32_bf16 v[180:183], v[36:39], v[80:83], v[180:183]\n"
"ds_read_b64_tr_b16 v[70:71], v3 offset:4608\n"
"v_mfma_f32_16x16x32_bf16 v[228:231], v[36:39], v[84:87], v[228:231]\n"
"ds_read_b64_tr_b16 v[72:73], v4 offset:8192\n"
"v_mfma_f32_16x16x32_bf16 v[132:135], v[76:79], v[36:39], v[132:135]\n"
"v_mfma_f32_16x16x32_bf16 v[184:187], v[40:43], v[80:83], v[184:187]\n"
"ds_read_b64_tr_b16 v[74:75], v4 offset:8704\n"
"v_mfma_f32_16x16x32_bf16 v[232:235], v[40:43], v[84:87], v[232:235]\n"
"ds_read_b128 v[20:23], v1 offset:2048\n"
"v_mfma_f32_16x16x32_bf16 v[136:139], v[76:79], v[40:43], v[136:139]\n"
"v_mfma_f32_16x16x32_bf16 v[188:191], v[44:47], v[80:83], v[188:191]\n"
"ds_read_b128 v[24:27], v1 offset:4096\n"
"v_mfma_f32_16x16x32_bf16 v[236:239], v[44:47], v[84:87], v[236:239]\n"
"ds_read_b128 v[28:31], v1 offset:6144\n"
"v_mfma_f32_16x16x32_bf16 v[140:143], v[76:79], v[44:47], v[140:143]\n"
"v_mfma_f32_16x16x32_bf16 v[192:195], v[48:51], v[80:83], v[192:195]\n"
"ds_read_b128 v[32:35], v1 offset:8192\n"
"v_mfma_f32_16x16x32_bf16 v[240:243], v[48:51], v[84:87], v[240:243]\n"
"ds_read_b128 v[36:39], v1 offset:10240\n"
"v_mfma_f32_16x16x32_bf16 v[144:147], v[76:79], v[48:51], v[144:147]\n"
"v_mfma_f32_16x16x32_bf16 v[196:199], v[52:55], v[80:83], v[196:199]\n"
"ds_read_b128 v[40:43], v1 offset:12288\n"
"v_mfma_f32_16x16x32_bf16 v[244:247], v[52:55], v[84:87], v[244:247]\n"
"ds_read_b128 v[44:47], v1 offset:14336\n"
"v_mfma_f32_16x16x32_bf16 v[148:151], v[76:79], v[52:55], v[148:151]\n"
"v_mfma_f32_16x16x32_bf16 v[200:203], v[56:59], v[80:83], v[200:203]\n"
"ds_read_b128 v[48:51], v1 offset:16384\n"
"v_mfma_f32_16x16x32_bf16 v[248:251], v[56:59], v[84:87], v[248:251]\n"
"ds_read_b128 v[52:55], v1 offset:18432\n"
"v_mfma_f32_16x16x32_bf16 v[152:155], v[76:79], v[56:59], v[152:155]\n"
"v_mfma_f32_16x16x32_bf16 v[204:207], v[60:63], v[80:83], v[204:207]\n"
"ds_read_b128 v[56:59], v1 offset:20480\n"
"v_mfma_f32_16x16x32_bf16 v[252:255], v[60:63], v[84:87], v[252:255]\n"
"v_mfma_f32_16x16x32_bf16 v[156:159], v[76:79], v[60:63], v[156:159]\n"
"s_cbranch_scc1 .Lfb_C_LOOPQ\n"
".Lfb_C_LEQ:\n"
"s_waitcnt lgkmcnt(8)\n"
"v_mfma_f32_16x16x32_bf16 v[160:163], v[16:19], v[68:71], v[160:163]\n"
"ds_read_b128 v[60:63], v1 offset:22528\n"
"v_mfma_f32_16x16x32_bf16 v[208:211], v[16:19], v[72:75], v[208:211]\n"
"v_mfma_f32_16x16x32_bf16 v[112:115], v[64:67], v[16:19], v[112:115]\n"
"v_mfma_f32_16x16x32_bf16 v[164:167], v[20:23], v[68:71], v[164:167]\n"
"ds_read_b128 v[16:19], v2 offset:0\n"
"v_mfma_f32_16x16x32_bf16 v[212:215], v[20:23], v[72:75], v[212:215]\n"
"ds_read_b64_tr_b16 v[76:77], v3 offset:12288\n"
"v_mfma_f32_16x16x32_bf16 v[116:119], v[64:67], v[20:23], v[116:119]\n"
"v_mfma_f32_16x16x32_bf16 v[168:171], v[24:27], v[68:71], v[168:171]\n"
"ds_read_b128 v[20:23], v2 offset:2048\n"
"v_mfma_f32_16x16x32_bf16 v[216:219], v[24:27], v[72:75], v[216:219]\n"
"ds_read_b64_tr_b16 v[78:79], v3 offset:12800\n"
"v_mfma_f32_16x16x32_bf16 v[120:123], v[64:67], v[24:27], v[120:123]\n"
"s_waitcnt lgkmcnt(10)\n"
"s_nop 0\n"
"v_mfma_f32_16x16x32_bf16 v[172:175], v[28:31], v[68:71], v[172:175]\n"
"ds_read_b128 v[24:27], v2 offset:4096\n"
"v_mfma_f32_16x16x32_bf16 v[220:223], v[28:31], v[72:75], v[220:223]\n"
"ds_read_b64_tr_b16 v[80:81], v3 offset:16384\n"
"v_mfma_f32_16x16x32_bf16 v[124:127], v[64:67], v[28:31], v[124:127]\n"
"v_mfma_f32_16x16x32_bf16 v[176:179], v[32:35], v[68:71], v[176:179]\n"
"ds_read_b128 v[28:31], v2 offset:6144\n"
"v_mfma_f32_16x16x32_bf16 v[224:227], v[32:35], v[72:75], v[224:227]\n"
"ds_read_b64_tr_b16 v[82:83], v3 offset:16896\n"
"v_mfma_f32_16x16x32_bf16 v[128:131], v[64:67], v[32:35], v[128:131]\n"
"v_mfma_f32_16x16x32_bf16 v[180:183], v[36:39], v[68:71], v[180:183]\n"
"ds_read_b128 v[32:35], v2 offset:8192\n"
"v_mfma_f32_16x16x32_bf16 v[228:231], v[36:39], v[72:75], v[228:231]\n"
"ds_read_b64_tr_b16 v[84:85], v4 offset:20480\n"
"v_mfma_f32_16x16x32_bf16 v[132:135], v[64:67], v[36:39], v[132:135]\n"
"s_waitcnt lgkmcnt(13)\n"
"s_nop 0\n"
"v_mfma_f32_16x16x32_bf16 v[184:187], v[40:43], v[68:71], v[184:187]\n"
"ds_read_b128 v[36:39], v2 offset:10240\n"
"v_mfma_f32_16x16x32_bf16 v[232:235], v[40:43], v[72:75], v[232:235]\n"
"ds_read_b64_tr_b16 v[86:87], v4 offset:20992\n"
"v_mfma_f32_16x16x32_bf16 v[136:139], v[64:67], v[40:43], v[136:139]\n"
"v_mfma_f32_16x16x32_bf16 v[188:191], v[44:47], v[68:71], v[188:191]\n"
"ds_read_b128 v[40:43], v2 offset:12288\n"
"v_mfma_f32_16x16x32_bf16 v[236:239], v[44:47], v[72:75], v[236:239]\n"
"v_mfma_f32_16x16x32_bf16 v[140:143], v[64:67], v[44:47], v[140:143]\n"
"v_mfma_f32_16x16x32_bf16 v[192:195], v[48:51], v[68:71], v[192:195]\n"
"ds_read_b128 v[44:47], v2 offset:14336\n"
"v_mfma_f32_16x16x32_bf16 v[240:243], v[48:51], v[72:75], v[240:243]\n"
"v_mfma_f32_16x16x32_bf16 v[144:147], v[64:67], v[48:51], v[144:147]\n"
"s_waitcnt lgkmcnt(14)\n"
"s_nop 0\n"
"v_mfma_f32_16x16x32_bf16 v[196:199], v[52:55], v[68:71], v[196:199]\n"
"ds_read_b128 v[48:51], v2 offset:16384\n"
"v_mfma_f32_16x16x32_bf16 v[244:247], v[52:55], v[72:75], v[244:247]\n"
"ds_read_b64 v[10:11], v9 offset:32\n"
"v_mfma_f32_16x16x32_bf16 v[148:151], v[64:67], v[52:55], v[148:151]\n"
"ds_read_b64 v[88:89], v7 offset:16\n"
"v_mfma_f32_16x16x32_bf16 v[200:203], v[56:59], v[68:71], v[200:203]\n"
"ds_read_b128 v[52:55], v2 offset:18432\n"
"v_mfma_f32_16x16x32_bf16 v[248:251], v[56:59], v[72:75], v[248:251]\n"
"ds_read_b64 v[90:91], v7 offset:528\n"
"v_mfma_f32_16x16x32_bf16 v[152:155], v[64:67], v[56:59], v[152:155]\n"
"ds_read_b64 v[92:93], v7 offset:1040\n"
"v_mfma_f32_16x16x32_bf16 v[204:207], v[60:63], v[68:71], v[204:207]\n"
"ds_read_b128 v[56:59], v2 offset:20480\n"
"v_mfma_f32_16x16x32_bf16 v[252:255], v[60:63], v[72:75], v[252:255]\n"
"ds_read_b64 v[94:95], v7 offset:1552\n"
"v_mfma_f32_16x16x32_bf16 v[156:159], v[64:67], v[60:63], v[156:159]\n"
"ds_read_b64 v[96:97], v7 offset:2064\n"
"s_waitcnt lgkmcnt(5)\n"
"s_nop 0\n"
"v_mfma_f32_16x16x32_bf16 v[160:163], v[16:19], v[80:83], v[160:163]\n"
"ds_read_b128 v[60:63], v2 offset:22528\n"
"v_mfma_f32_16x16x32_bf16 v[208:211], v[16:19], v[84:87], v[208:211]\n"
"ds_read_b64 v[98:99], v7 offset:2576\n"
"v_mfma_f32_16x16x32_bf16 v[112:115], v[76:79], v[16:19], v[112:115]\n"
"ds_read_b64 v[100:101], v7 offset:3088\n"
"v_mfma_f32_16x16x32_bf16 v[164:167], v[20:23], v[80:83], v[164:167]\n"
"ds_read_b64 v[102:103], v7 offset:3600\n"
"v_mfma_f32_16x16x32_bf16 v[212:215], v[20:23], v[84:87], v[212:215]\n"
"ds_read_b64 v[104:105], v7 offset:4112\n"
"v_mfma_f32_16x16x32_bf16 v[116:119], v[76:79], v[20:23], v[116:119]\n"
"ds_read_b64 v[106:107], v7 offset:4624\n"
"v_mfma_f32_16x16x32_bf16 v[168:171], v[24:27], v[80:83], v[168:171]\n"
"ds_read_b64 v[108:109], v7 offset:5136\n"
"v_mfma_f32_16x16x32_bf16 v[216:219], v[24:27], v[84:87], v[216:219]\n"
"ds_read_b64 v[110:111], v7 offset:5648\n"
"v_mfma_f32_16x16x32_bf16 v[120:123], v[76:79], v[24:27], v[120:123]\n"
"s_waitcnt lgkmcnt(7)\n"
"v_mfma_f32_16x16x32_bf16 v[172:175], v[28:31], v[80:83], v[172:175]\n"
"ds_read2st64_b32 v[64:65], v6 offset0:0 offset1:4\n"
"v_mfma_f32_16x16x32_bf16 v[220:223], v[28:31], v[84:87], v[220:223]\n"
"v_add_u32 v6, 4, v6\n"
"v_mfma_f32_16x16x32_bf16 v[124:127], v[76:79], v[28:31], v[124:127]\n"
"v_mfma_f32_16x16x32_bf16 v[176:179], v[32:35], v[80:83], v[176:179]\n"
"ds_read2st64_b32 v[66:67], v6 offset0:0 offset1:4\n"
"v_mfma_f32_16x16x32_bf16 v[224:227], v[32:35], v[84:87], v[224:227]\n"
"v_add_u32_e64 v6, 4, v6\n"
"v_mfma_f32_16x16x32_bf16 v[128:131], v[76:79], v[32:35], v[128:131]\n"
"v_readfirstlane_b32 s32, v10\n"
"v_mfma_f32_16x16x32_bf16 v[180:183], v[36:39], v[80:83], v[180:183]\n"
"ds_read2st64_b32 v[68:69], v6 offset0:0 offset1:4\n"
"v_mfma_f32_16x16x32_bf16 v[228:231], v[36:39], v[84:87], v[228:231]\n"
"v_readfirstlane_b32 s33, v11\n"
"v_mfma_f32_16x16x32_bf16 v[132:135], v[76:79], v[36:39], v[132:135]\n"
"v_mfma_f32_16x16x32_bf16 v[184:187], v[40:43], v[80:83], v[184:187]\n"
"ds_read2_b32 v[16:17], v8 offset0:6 offset1:14\n"
"ds_read2_b32 v[18:19], v8 offset0:22 offset1:30\n"
"v_mfma_f32_16x16x32_bf16 v[232:235], v[40:43], v[84:87], v[232:235]\n"
"ds_read2_b32 v[20:21], v8 offset0:134 offset1:142\n"
"ds_read2_b32 v[22:23], v8 offset0:150 offset1:158\n"
"v_mfma_f32_16x16x32_bf16 v[136:139], v[76:79], v[40:43], v[136:139]\n"
"v_add_u32 v8, 1024, v8\n"
"v_mfma_f32_16x16x32_bf16 v[188:191], v[44:47], v[80:83], v[188:191]\n"
"ds_read2_b32 v[24:25], v8 offset0:6 offset1:14\n"
"ds_read2_b32 v[26:27], v8 offset0:22 offset1:30\n"
"v_mfma_f32_16x16x32_bf16 v[236:239], v[44:47], v[84:87], v[236:239]\n"
"ds_read2_b32 v[28:29], v8 offset0:134 offset1:142\n"
"ds_read2_b32 v[30:31], v8 offset0:150 offset1:158\n"
"v_mfma_f32_16x16x32_bf16 v[140:143], v[76:79], v[44:47], v[140:143]\n"
"v_add_u32 v8, 1024, v8\n"
"v_mfma_f32_16x16x32_bf16 v[192:195], v[48:51], v[80:83], v[192:195]\n"
"ds_read2_b32 v[32:33], v8 offset0:6 offset1:14\n"
"ds_read2_b32 v[34:35], v8 offset0:22 offset1:30\n"
"v_mfma_f32_16x16x32_bf16 v[240:243], v[48:51], v[84:87], v[240:243]\n"
"ds_read2_b32 v[36:37], v8 offset0:134 offset1:142\n"
"ds_read2_b32 v[38:39], v8 offset0:150 offset1:158\n"
"v_mfma_f32_16x16x32_bf16 v[144:147], v[76:79], v[48:51], v[144:147]\n"
"v_add_u32 v8, 1024, v8\n"
"v_mfma_f32_16x16x32_bf16 v[196:199], v[52:55], v[80:83], v[196:199]\n"
"ds_read2_b32 v[40:41], v8 offset0:6 offset1:14\n"
"ds_read2_b32 v[42:43], v8 offset0:22 offset1:30\n"
"v_mfma_f32_16x16x32_bf16 v[244:247], v[52:55], v[84:87], v[244:247]\n"
"ds_read2_b32 v[44:45], v8 offset0:134 offset1:142\n"
"ds_read2_b32 v[46:47], v8 offset0:150 offset1:158\n"
"v_mfma_f32_16x16x32_bf16 v[148:151], v[76:79], v[52:55], v[148:151]\n"
"v_add_u32 v8, 1024, v8\n"
"v_mfma_f32_16x16x32_bf16 v[200:203], v[56:59], v[80:83], v[200:203]\n"
"v_mov_b32 v72, 0x3f803f80\n"
"v_mfma_f32_16x16x32_bf16 v[248:251], v[56:59], v[84:87], v[248:251]\n"
"v_mov_b32 v73, 0x3f803f80\n"
"v_mfma_f32_16x16x32_bf16 v[152:155], v[76:79], v[56:59], v[152:155]\n"
"v_mfma_f32_16x16x32_bf16 v[204:207], v[60:63], v[80:83], v[204:207]\n"
"v_mov_b32 v74, 0x3f803f80\n"
"v_mfma_f32_16x16x32_bf16 v[252:255], v[60:63], v[84:87], v[252:255]\n"
"v_mov_b32 v75, 0x3f803f80\n"
"v_mfma_f32_16x16x32_bf16 v[156:159], v[76:79], v[60:63], v[156:159]\n"
"s_waitcnt lgkmcnt(0)\n"
"s_nop 0\n"
"v_mfma_f32_16x16x16_bf16 v[208:211], v[88:89], v[68:69], v[208:211]\n"
"ds_read2_b32 v[48:49], v8 offset0:6 offset1:14\n"
"ds_read2_b32 v[50:51], v8 offset0:22 offset1:30\n"
"ds_read2_b32 v[52:53], v8 offset0:134 offset1:142\n"
"ds_read2_b32 v[54:55], v8 offset0:150 offset1:158\n"
"v_mfma_f32_16x16x16_bf16 v[212:215], v[90:91], v[68:69], v[212:215]\n"
"v_add_u32 v8, 1024, v8\n"
"ds_read2_b32 v[56:57], v8 offset0:6 offset1:14\n"
"ds_read2_b32 v[58:59], v8 offset0:22 offset1:30\n"
"ds_read2_b32 v[60:61], v8 offset0:134 offset1:142\n"
"v_mfma_f32_16x16x16_bf16 v[160:163], v[88:89], v[66:67], v[160:163]\n"
"ds_read2_b32 v[62:63], v8 offset0:150 offset1:158\n"
"v_lshl_add_u32 v70, s20, 2, v9\n"
"v_mov_b32 v71, 3\n"
"v_add_u32 v1, s34, v5\n"
"v_mfma_f32_16x16x16_bf16 v[164:167], v[90:91], v[66:67], v[164:167]\n"
"v_add_u32_e64 v2, s35, v0\n"
"v_lshl_add_u32 v4, v0, 3, s35\n"
"v_lshl_add_u32 v3, v0, 7, s36\n"
"v_pk_mul_f32 v[208:209], v[208:209], v[16:17]\n"
"v_pk_mul_f32 v[210:211], v[210:211], v[18:19]\n"
"v_pk_mul_f32 v[212:213], v[212:213], v[20:21]\n"
"v_pk_mul_f32 v[214:215], v[214:215], v[22:23]\n"
"ds_write_b32 v70, v71 offset:16\n"
"v_cvt_pk_bf16_f32 v208, v208, v209\n"
"v_cvt_pk_bf16_f32 v209, v210, v211\n"
"v_cvt_pk_bf16_f32 v210, v212, v213\n"
"v_cvt_pk_bf16_f32 v211, v214, v215\n"
"v_pk_fma_f32 v[160:161], v[160:161], v[16:17], s[60:61] op_sel:[0,0,0] op_sel_hi:[1,1,0]\n"
"v_pk_fma_f32 v[162:163], v[162:163], v[18:19], s[60:61] op_sel:[0,0,0] op_sel_hi:[1,1,0]\n"
"v_pk_fma_f32 v[164:165], v[164:165], v[20:21], s[60:61] op_sel:[0,0,0] op_sel_hi:[1,1,0]\n"
"v_pk_fma_f32 v[166:167], v[166:167], v[22:23], s[60:61] op_sel:[0,0,0] op_sel_hi:[1,1,0]\n"
"v_pk_fma_f32 v[76:77], v[160:161], s[62:63], s[62:63] op_sel:[0,0,1] op_sel_hi:[1,0,1]\n"
"v_pk_fma_f32 v[78:79], v[162:163], s[62:63], s[62:63] op_sel:[0,0,1] op_sel_hi:[1,0,1]\n"
"v_pk_fma_f32 v[80:81], v[164:165], s[62:63], s[62:63] op_sel:[0,0,1] op_sel_hi:[1,0,1]\n"
"v_pk_fma_f32 v[82:83], v[166:167], s[62:63], s[62:63] op_sel:[0,0,1] op_sel_hi:[1,0,1]\n"
"v_mfma_f32_16x16x16_bf16 v[216:219], v[92:93], v[68:69], v[216:219]\n"
"v_exp_f32_e64 v76, v76 clamp\n"
"v_exp_f32_e64 v77, v77 clamp\n"
"v_mfma_f32_16x16x16_bf16 v[220:223], v[94:95], v[68:69], v[220:223]\n"
"v_exp_f32_e64 v78, v78 clamp\n"
"v_exp_f32_e64 v79, v79 clamp\n"
"v_mfma_f32_16x16x16_bf16 v[168:171], v[92:93], v[66:67], v[168:171]\n"
"v_exp_f32_e64 v80, v80 clamp\n"
"v_exp_f32_e64 v81, v81 clamp\n"
"v_mfma_f32_16x16x16_bf16 v[172:175], v[94:95], v[66:67], v[172:175]\n"
"v_exp_f32_e64 v82, v82 clamp\n"
"v_exp_f32_e64 v83, v83 clamp\n"
"v_max_f32 v160, s60, v160\n"
"v_max_f32 v161, s60, v161\n"
"v_max_f32 v162, s60, v162\n"
"v_max_f32 v163, s60, v163\n"
"v_max_f32 v164, s60, v164\n"
"v_max_f32 v165, s60, v165\n"
"v_max_f32 v166, s60, v166\n"
"v_max_f32 v167, s60, v167\n"
"v_pk_add_f32 v[76:77], v[76:77], v[160:161]\n"
"v_pk_add_f32 v[78:79], v[78:79], v[162:163]\n"
"v_pk_add_f32 v[80:81], v[80:81], v[164:165]\n"
"v_pk_add_f32 v[82:83], v[82:83], v[166:167]\n"
"v_cvt_pk_bf16_f32 v160, v76, v77\n"
"v_cvt_pk_bf16_f32 v161, v78, v79\n"
"v_cvt_pk_bf16_f32 v162, v80, v81\n"
"v_cvt_pk_bf16_f32 v163, v82, v83\n"
"v_pk_mul_f32 v[216:217], v[216:217], v[24:25]\n"
"v_pk_mul_f32 v[218:219], v[218:219], v[26:27]\n"
"v_pk_mul_f32 v[220:221], v[220:221], v[28:29]\n"
"v_pk_mul_f32 v[222:223], v[222:223], v[30:31]\n"
"v_cvt_pk_bf16_f32 v216, v216, v217\n"
"v_cvt_pk_bf16_f32 v217, v218, v219\n"
"v_cvt_pk_bf16_f32 v218, v220, v221\n"
"v_cvt_pk_bf16_f32 v219, v222, v223\n"
"v_pk_fma_f32 v[168:169], v[168:169], v[24:25], s[60:61] op_sel:[0,0,0] op_sel_hi:[1,1,0]\n"
"v_pk_fma_f32 v[170:171], v[170:171], v[26:27], s[60:61] op_sel:[0,0,0] op_sel_hi:[1,1,0]\n"
"v_pk_fma_f32 v[172:173], v[172:173], v[28:29], s[60:61] op_sel:[0,0,0] op_sel_hi:[1,1,0]\n"
"v_pk_fma_f32 v[174:175], v[174:175], v[30:31], s[60:61] op_sel:[0,0,0] op_sel_hi:[1,1,0]\n"
"v_pk_fma_f32 v[76:77], v[168:169], s[62:63], s[62:63] op_sel:[0,0,1] op_sel_hi:[1,0,1]\n"
"v_pk_fma_f32 v[78:79], v[170:171], s[62:63], s[62:63] op_sel:[0,0,1] op_sel_hi:[1,0,1]\n"
"v_pk_fma_f32 v[80:81], v[172:173], s[62:63], s[62:63] op_sel:[0,0,1] op_sel_hi:[1,0,1]\n"
"v_pk_fma_f32 v[82:83], v[174:175], s[62:63], s[62:63] op_sel:[0,0,1] op_sel_hi:[1,0,1]\n"
"v_mfma_f32_16x16x16_bf16 v[224:227], v[96:97], v[68:69], v[224:227]\n"
"v_exp_f32_e64 v76, v76 clamp\n"
"v_exp_f32_e64 v77, v77 clamp\n"
"v_mfma_f32_16x16x16_bf16 v[228:231], v[98:99], v[68:69], v[228:231]\n"
"v_exp_f32_e64 v78, v78 clamp\n"
"v_exp_f32_e64 v79, v79 clamp\n"
"v_mfma_f32_16x16x16_bf16 v[176:179], v[96:97], v[66:67], v[176:179]\n"
"v_exp_f32_e64 v80, v80 clamp\n"
"v_exp_f32_e64 v81, v81 clamp\n"
"v_mfma_f32_16x16x16_bf16 v[180:183], v[98:99], v[66:67], v[180:183]\n"
"v_exp_f32_e64 v82, v82 clamp\n"
"v_exp_f32_e64 v83, v83 clamp\n"
"v_max_f32_e64 v168, s60, v168\n"
"v_mfma_f32_16x16x32_bf16 v[84:87], v[160:163], v[208:211], 0\n"
"v_max_f32 v169, s60, v169\n"
"v_max_f32 v170, s60, v170\n"
"v_max_f32_e64 v171, s60, v171\n"
"v_mfma_f32_16x16x32_bf16 v[12:15], v[160:163], v[72:75], 0\n"
"v_max_f32 v172, s60, v172\n"
"v_max_f32 v173, s60, v173\n"
"v_max_f32 v174, s60, v174\n"
"v_max_f32 v175, s60, v175\n"
"v_pk_add_f32 v[76:77], v[76:77], v[168:169]\n"
"v_pk_add_f32 v[78:79], v[78:79], v[170:171]\n"
"v_pk_add_f32 v[80:81], v[80:81], v[172:173]\n"
"v_pk_add_f32 v[82:83], v[82:83], v[174:175]\n"
"v_cvt_pk_bf16_f32 v168, v76, v77\n"
"v_cvt_pk_bf16_f32 v169, v78, v79\n"
"v_cvt_pk_bf16_f32 v170, v80, v81\n"
"v_cvt_pk_bf16_f32 v171, v82, v83\n"
"v_pk_mul_f32 v[224:225], v[224:225], v[32:33]\n"
"v_pk_mul_f32 v[226:227], v[226:227], v[34:35]\n"
"v_pk_mul_f32 v[228:229], v[228:229], v[36:37]\n"
"v_pk_mul_f32 v[230:231], v[230:231], v[38:39]\n"
"v_cvt_pk_bf16_f32 v224, v224, v225\n"
"v_cvt_pk_bf16_f32 v225, v226, v227\n"
"v_cvt_pk_bf16_f32 v226, v228, v229\n"
"v_cvt_pk_bf16_f32 v227, v230, v231\n"
"v_pk_fma_f32 v[176:177], v[176:177], v[32:33], s[60:61] op_sel:[0,0,0] op_sel_hi:[1,1,0]\n"
"v_pk_fma_f32 v[178:179], v[178:179], v[34:35], s[60:61] op_sel:[0,0,0] op_sel_hi:[1,1,0]\n"
"v_pk_fma_f32 v[180:181], v[180:181], v[36:37], s[60:61] op_sel:[0,0,0] op_sel_hi:[1,1,0]\n"
"v_pk_fma_f32 v[182:183], v[182:183], v[38:39], s[60:61] op_sel:[0,0,0] op_sel_hi:[1,1,0]\n"
"v_pk_fma_f32 v[76:77], v[176:177], s[62:63], s[62:63] op_sel:[0,0,1] op_sel_hi:[1,0,1]\n"
"v_pk_fma_f32 v[78:79], v[178:179], s[62:63], s[62:63] op_sel:[0,0,1] op_sel_hi:[1,0,1]\n"
"v_pk_fma_f32 v[80:81], v[180:181], s[62:63], s[62:63] op_sel:[0,0,1] op_sel_hi:[1,0,1]\n"
"v_pk_fma_f32 v[82:83], v[182:183], s[62:63], s[62:63] op_sel:[0,0,1] op_sel_hi:[1,0,1]\n"
"v_mfma_f32_16x16x16_bf16 v[232:235], v[100:101], v[68:69], v[232:235]\n"
"v_exp_f32_e64 v76, v76 clamp\n"
"v_exp_f32_e64 v77, v77 clamp\n"
"v_mfma_f32_16x16x16_bf16 v[236:239], v[102:103], v[68:69], v[236:239]\n"
"v_exp_f32_e64 v78, v78 clamp\n"
"v_exp_f32_e64 v79, v79 clamp\n"
"v_mfma_f32_16x16x16_bf16 v[184:187], v[100:101], v[66:67], v[184:187]\n"
"v_exp_f32_e64 v80, v80 clamp\n"
"v_exp_f32_e64 v81, v81 clamp\n"
"v_mfma_f32_16x16x16_bf16 v[188:191], v[102:103], v[66:67], v[188:191]\n"
"v_exp_f32_e64 v82, v82 clamp\n"
"v_exp_f32_e64 v83, v83 clamp\n"
"v_max_f32_e64 v176, s60, v176\n"
"v_mfma_f32_16x16x32_bf16 v[84:87], v[168:171], v[216:219], v[84:87]\n"
"v_max_f32 v177, s60, v177\n"
"v_max_f32 v178, s60, v178\n"
"v_max_f32_e64 v179, s60, v179\n"
"v_mfma_f32_16x16x32_bf16 v[12:15], v[168:171], v[72:75], v[12:15]\n"
"v_max_f32 v180, s60, v180\n"
"v_max_f32 v181, s60, v181\n"
"v_max_f32 v182, s60, v182\n"
"v_max_f32 v183, s60, v183\n"
"v_pk_add_f32 v[76:77], v[76:77], v[176:177]\n"
"v_pk_add_f32 v[78:79], v[78:79], v[178:179]\n"
"v_pk_add_f32 v[80:81], v[80:81], v[180:181]\n"
"v_pk_add_f32 v[82:83], v[82:83], v[182:183]\n"
"v_cvt_pk_bf16_f32 v176, v76, v77\n"
"v_cvt_pk_bf16_f32 v177, v78, v79\n"
"v_cvt_pk_bf16_f32 v178, v80, v81\n"
"v_cvt_pk_bf16_f32 v179, v82, v83\n"
"v_pk_mul_f32 v[232:233], v[232:233], v[40:41]\n"
"v_pk_mul_f32 v[234:235], v[234:235], v[42:43]\n"
"v_pk_mul_f32 v[236:237], v[236:237], v[44:45]\n"
"v_pk_mul_f32 v[238:239], v[238:239], v[46:47]\n"
"v_cvt_pk_bf16_f32 v232, v232, v233\n"
"v_cvt_pk_bf16_f32 v233, v234, v235\n"
"v_cvt_pk_bf16_f32 v234, v236, v237\n"
"v_cvt_pk_bf16_f32 v235, v238, v239\n"
"v_pk_fma_f32 v[184:185], v[184:185], v[40:41], s[60:61] op_sel:[0,0,0] op_sel_hi:[1,1,0]\n"
"v_pk_fma_f32 v[186:187], v[186:187], v[42:43], s[60:61] op_sel:[0,0,0] op_sel_hi:[1,1,0]\n"
"v_pk_fma_f32 v[188:189], v[188:189], v[44:45], s[60:61] op_sel:[0,0,0] op_sel_hi:[1,1,0]\n"
"v_pk_fma_f32 v[190:191], v[190:191], v[46:47], s[60:61] op_sel:[0,0,0] op_sel_hi:[1,1,0]\n"
"v_pk_fma_f32 v[76:77], v[184:185], s[62:63], s[62:63] op_sel:[0,0,1] op_sel_hi:[1,0,1]\n"
"v_pk_fma_f32 v[78:79], v[186:187], s[62:63], s[62:63] op_sel:[0,0,1] op_sel_hi:[1,0,1]\n"
"v_pk_fma_f32 v[80:81], v[188:189], s[62:63], s[62:63] op_sel:[0,0,1] op_sel_hi:[1,0,1]\n"
"v_pk_fma_f32 v[82:83], v[190:191], s[62:63], s[62:63] op_sel:[0,0,1] op_sel_hi:[1,0,1]\n"
"v_mfma_f32_16x16x16_bf16 v[240:243], v[104:105], v[68:69], v[240:243]\n"
"v_exp_f32_e64 v76, v76 clamp\n"
"v_exp_f32_e64 v77, v77 clamp\n"
"v_mfma_f32_16x16x16_bf16 v[244:247], v[106:107], v[68:69], v[244:247]\n"
"v_exp_f32_e64 v78, v78 clamp\n"
"v_exp_f32_e64 v79, v79 clamp\n"
"v_mfma_f32_16x16x16_bf16 v[192:195], v[104:105], v[66:67], v[192:195]\n"
"v_exp_f32_e64 v80, v80 clamp\n"
"v_exp_f32_e64 v81, v81 clamp\n"
"v_mfma_f32_16x16x16_bf16 v[196:199], v[106:107], v[66:67], v[196:199]\n"
"v_exp_f32_e64 v82, v82 clamp\n"
"v_exp_f32_e64 v83, v83 clamp\n"
"v_max_f32_e64 v184, s60, v184\n"
"v_mfma_f32_16x16x32_bf16 v[84:87], v[176:179], v[224:227], v[84:87]\n"
"v_max_f32 v185, s60, v185\n"
"v_max_f32 v186, s60, v186\n"
"v_max_f32_e64 v187, s60, v187\n"
"v_mfma_f32_16x16x32_bf16 v[12:15], v[176:179], v[72:75], v[12:15]\n"
"v_max_f32 v188, s60, v188\n"
"v_max_f32 v189, s60, v189\n"
"v_max_f32 v190, s60, v190\n"
"v_max_f32 v191, s60, v191\n"
"v_pk_add_f32 v[76:77], v[76:77], v[184:185]\n"
"v_pk_add_f32 v[78:79], v[78:79], v[186:187]\n"
"v_pk_add_f32 v[80:81], v[80:81], v[188:189]\n"
"v_pk_add_f32 v[82:83], v[82:83], v[190:191]\n"
"v_cvt_pk_bf16_f32 v184, v76, v77\n"
"v_cvt_pk_bf16_f32 v185, v78, v79\n"
"v_cvt_pk_bf16_f32 v186, v80, v81\n"
"v_cvt_pk_bf16_f32 v187, v82, v83\n"
"s_nop 0\n"
"s_nop 0\n"
"v_mfma_f32_16x16x32_bf16 v[84:87], v[184:187], v[232:235], v[84:87]\n"
"v_mfma_f32_16x16x32_bf16 v[12:15], v[184:187], v[72:75], v[12:15]\n"
"s_cmp_eq_u32 s21, 10\n"
"s_cbranch_scc1 .Lfb_EQ_KD\n"
"s_waitcnt lgkmcnt(0)\n"
"s_nop 0\n"
"v_pk_mul_f32 v[240:241], v[240:241], v[48:49]\n"
"v_pk_mul_f32 v[242:243], v[242:243], v[50:51]\n"
"v_pk_mul_f32 v[244:245], v[244:245], v[52:53]\n"
"v_pk_mul_f32 v[246:247], v[246:247], v[54:55]\n"
"v_cvt_pk_bf16_f32 v240, v240, v241\n"
"v_cvt_pk_bf16_f32 v241, v242, v243\n"
"v_cvt_pk_bf16_f32 v242, v244, v245\n"
"v_cvt_pk_bf16_f32 v243, v246, v247\n"
"v_pk_fma_f32 v[192:193], v[192:193], v[48:49], s[60:61] op_sel:[0,0,0] op_sel_hi:[1,1,0]\n"
"v_pk_fma_f32 v[194:195], v[194:195], v[50:51], s[60:61] op_sel:[0,0,0] op_sel_hi:[1,1,0]\n"
"v_pk_fma_f32 v[196:197], v[196:197], v[52:53], s[60:61] op_sel:[0,0,0] op_sel_hi:[1,1,0]\n"
"v_pk_fma_f32 v[198:199], v[198:199], v[54:55], s[60:61] op_sel:[0,0,0] op_sel_hi:[1,1,0]\n"
"v_pk_fma_f32 v[76:77], v[192:193], s[62:63], s[62:63] op_sel:[0,0,1] op_sel_hi:[1,0,1]\n"
"v_pk_fma_f32 v[78:79], v[194:195], s[62:63], s[62:63] op_sel:[0,0,1] op_sel_hi:[1,0,1]\n"
"v_pk_fma_f32 v[80:81], v[196:197], s[62:63], s[62:63] op_sel:[0,0,1] op_sel_hi:[1,0,1]\n"
"v_pk_fma_f32 v[82:83], v[198:199], s[62:63], s[62:63] op_sel:[0,0,1] op_sel_hi:[1,0,1]\n"
"v_mfma_f32_16x16x16_bf16 v[248:251], v[108:109], v[68:69], v[248:251]\n"
"v_exp_f32_e64 v76, v76 clamp\n"
"v_exp_f32_e64 v77, v77 clamp\n"
"v_mfma_f32_16x16x16_bf16 v[252:255], v[110:111], v[68:69], v[252:255]\n"
"v_exp_f32_e64 v78, v78 clamp\n"
"v_exp_f32_e64 v79, v79 clamp\n"
"v_mfma_f32_16x16x16_bf16 v[200:203], v[108:109], v[66:67], v[200:203]\n"
"v_exp_f32_e64 v80, v80 clamp\n"
"v_exp_f32_e64 v81, v81 clamp\n"
"v_mfma_f32_16x16x16_bf16 v[204:207], v[110:111], v[66:67], v[204:207]\n"
"v_exp_f32_e64 v82, v82 clamp\n"
"v_exp_f32_e64 v83, v83 clamp\n"
"v_max_f32 v192, s60, v192\n"
"v_max_f32 v193, s60, v193\n"
"v_max_f32 v194, s60, v194\n"
"v_max_f32 v195, s60, v195\n"
"v_max_f32 v196, s60, v196\n"
"v_max_f32 v197, s60, v197\n"
"v_max_f32 v198, s60, v198\n"
"v_max_f32 v199, s60, v199\n"
"v_pk_add_f32 v[76:77], v[76:77], v[192:193]\n"
"v_pk_add_f32 v[78:79], v[78:79], v[194:195]\n"
"v_pk_add_f32 v[80:81], v[80:81], v[196:197]\n"
"v_pk_add_f32 v[82:83], v[82:83], v[198:199]\n"
"v_cvt_pk_bf16_f32 v192, v76, v77\n"
"v_cvt_pk_bf16_f32 v193, v78, v79\n"
"v_cvt_pk_bf16_f32 v194, v80, v81\n"
"v_cvt_pk_bf16_f32 v195, v82, v83\n"
"v_pk_mul_f32 v[248:249], v[248:249], v[56:57]\n"
"v_pk_mul_f32 v[250:251], v[250:251], v[58:59]\n"
"v_pk_mul_f32 v[252:253], v[252:253], v[60:61]\n"
"v_pk_mul_f32 v[254:255], v[254:255], v[62:63]\n"
"v_cvt_pk_bf16_f32 v248, v248, v249\n"
"v_cvt_pk_bf16_f32 v249, v250, v251\n"
"v_cvt_pk_bf16_f32 v250, v252, v253\n"
"v_cvt_pk_bf16_f32 v251, v254, v255\n"
"v_pk_fma_f32 v[200:201], v[200:201], v[56:57], s[60:61] op_sel:[0,0,0] op_sel_hi:[1,1,0]\n"
"v_pk_fma_f32 v[202:203], v[202:203], v[58:59], s[60:61] op_sel:[0,0,0] op_sel_hi:[1,1,0]\n"
"v_pk_fma_f32 v[204:205], v[204:205], v[60:61], s[60:61] op_sel:[0,0,0] op_sel_hi:[1,1,0]\n"
"v_pk_fma_f32 v[206:207], v[206:207], v[62:63], s[60:61] op_sel:[0,0,0] op_sel_hi:[1,1,0]\n"
"v_pk_fma_f32 v[76:77], v[200:201], s[62:63], s[62:63] op_sel:[0,0,1] op_sel_hi:[1,0,1]\n"
"v_pk_fma_f32 v[78:79], v[202:203], s[62:63], s[62:63] op_sel:[0,0,1] op_sel_hi:[1,0,1]\n"
"v_pk_fma_f32 v[80:81], v[204:205], s[62:63], s[62:63] op_sel:[0,0,1] op_sel_hi:[1,0,1]\n"
"v_pk_fma_f32 v[82:83], v[206:207], s[62:63], s[62:63] op_sel:[0,0,1] op_sel_hi:[1,0,1]\n"
"v_exp_f32_e64 v76, v76 clamp\n"
"v_exp_f32_e64 v77, v77 clamp\n"
"v_exp_f32_e64 v78, v78 clamp\n"
"v_exp_f32_e64 v79, v79 clamp\n"
"v_exp_f32_e64 v80, v80 clamp\n"
"v_exp_f32_e64 v81, v81 clamp\n"
"v_exp_f32_e64 v82, v82 clamp\n"
"v_exp_f32_e64 v83, v83 clamp\n"
"v_max_f32_e64 v200, s60, v200\n"
"v_mfma_f32_16x16x32_bf16 v[84:87], v[192:195], v[240:243], v[84:87]\n"
"v_max_f32 v201, s60, v201\n"
"v_max_f32 v202, s60, v202\n"
"v_max_f32_e64 v203, s60, v203\n"
"v_mfma_f32_16x16x32_bf16 v[12:15], v[192:195], v[72:75], v[12:15]\n"
"v_max_f32 v204, s60, v204\n"
"v_max_f32 v205, s60, v205\n"
"v_max_f32 v206, s60, v206\n"
"v_max_f32 v207, s60, v207\n"
"v_pk_add_f32 v[76:77], v[76:77], v[200:201]\n"
"v_pk_add_f32 v[78:79], v[78:79], v[202:203]\n"
"v_pk_add_f32 v[80:81], v[80:81], v[204:205]\n"
"v_pk_add_f32 v[82:83], v[82:83], v[206:207]\n"
"v_cvt_pk_bf16_f32 v200, v76, v77\n"
"v_cvt_pk_bf16_f32 v201, v78, v79\n"
"v_cvt_pk_bf16_f32 v202, v80, v81\n"
"v_cvt_pk_bf16_f32 v203, v82, v83\n"
"s_nop 0\n"
"s_nop 0\n"
"v_mfma_f32_16x16x32_bf16 v[84:87], v[200:203], v[248:251], v[84:87]\n"
"v_mfma_f32_16x16x32_bf16 v[12:15], v[200:203], v[72:75], v[12:15]\n"
".Lfb_EQ_KD:\n"
"s_waitcnt lgkmcnt(0)\n"
"s_nop 0\n"
"s_nop 3\n"
"s_nop 0\n"
"v_perm_b32 v84, v84, v10, s70\n"
"v_perm_b32 v85, v85, v10, s71\n"
"global_store_dwordx4 v1, v[84:87], s[52:53] sc1\n"
"v_perm_b32 v12, v12, v10, s70\n"
"v_perm_b32 v13, v13, v10, s71\n"
"v_perm_b32 v14, v14, v10, s72\n"
"v_perm_b32 v15, v15, v10, s73\n"
"s_mov_b32 exec_lo, 0x00010001\n"
"s_mov_b32 exec_hi, 0x00010001\n"
"global_store_dwordx4 v2, v[12:15], s[54:55] sc1\n"
"s_mov_b64 exec, 0x7ff\n"
"global_atomic_swap_x2 v3, v[10:11], s[56:57] offset:0 sc1\n"
"s_mov_b64 exec, -1\n"
"s_nop 0\n"
"v_mfma_f32_16x16x16_bf16 v[112:115], v[64:65], v[88:89], v[112:115]\n"
"ds_read_b32 v16, v7 offset:24\n"
"ds_read_b32 v17, v7 offset:536\n"
"v_mfma_f32_16x16x16_bf16 v[116:119], v[64:65], v[90:91], v[116:119]\n"
"ds_read_b32 v18, v7 offset:1048\n"
"ds_read_b32 v19, v7 offset:1560\n"
"v_mfma_f32_16x16x16_bf16 v[120:123], v[64:65], v[92:93], v[120:123]\n"
"ds_read_b32 v20, v7 offset:2072\n"
"ds_read_b32 v21, v7 offset:2584\n"
"v_mfma_f32_16x16x16_bf16 v[124:127], v[64:65], v[94:95], v[124:127]\n"
"ds_read_b32 v22, v7 offset:3096\n"
"ds_read_b32 v23, v7 offset:3608\n"
"v_mfma_f32_16x16x16_bf16 v[128:131], v[64:65], v[96:97], v[128:131]\n"
"ds_read_b32 v24, v7 offset:4120\n"
"ds_read_b32 v25, v7 offset:4632\n"
"v_mfma_f32_16x16x16_bf16 v[132:135], v[64:65], v[98:99], v[132:135]\n"
"ds_read_b32 v26, v7 offset:5144\n"
"ds_read_b32 v27, v7 offset:5656\n"
"v_mfma_f32_16x16x16_bf16 v[136:139], v[64:65], v[100:101], v[136:139]\n"
"s_bfe_u32 s70, s32, 0x80000\n"
"s_bfe_u32 s71, s32, 0x80008\n"
"v_mfma_f32_16x16x16_bf16 v[140:143], v[64:65], v[102:103], v[140:143]\n"
"s_bfe_u32 s72, s32, 0x80010\n"
"s_bfe_u32 s73, s32, 0x80018\n"
"v_mfma_f32_16x16x16_bf16 v[144:147], v[64:65], v[104:105], v[144:147]\n"
"v_mov_b64 v[56:57], 0\n"
"v_and_b32 v9, 15, v0\n"
"v_mfma_f32_16x16x16_bf16 v[148:151], v[64:65], v[106:107], v[148:151]\n"
"v_and_b32_e64 v6, 48, v0\n"
"v_lshl_add_u32 v9, v9, 7, v6\n"
"v_mfma_f32_16x16x16_bf16 v[152:155], v[64:65], v[108:109], v[152:155]\n"
"v_add_u32 v8, 0x1000, v5\n"
"v_add_u32 v6, 0x2000, v5\n"
"v_mfma_f32_16x16x16_bf16 v[156:159], v[64:65], v[110:111], v[156:159]\n"
"v_mov_b32 v52, s70\n"
"v_mov_b32 v53, s71\n"
"v_mov_b32 v54, s72\n"
"v_mov_b32_e64 v55, s73\n"
"s_waitcnt lgkmcnt(0)\n"
"s_mov_b64 exec, 0x7ff\n"
"global_atomic_or_x2 v[58:59], v4, v[56:57], s[56:57] offset:0 sc0 sc1\n"
"s_mov_b64 exec, -1\n"
"s_nop 0\n"
"v_pk_fma_f32 v[112:113], v[112:113], v[16:17], s[60:61] op_sel:[0,0,0] op_sel_hi:[1,0,0]\n"
"v_pk_fma_f32 v[114:115], v[114:115], v[16:17], s[60:61] op_sel:[0,0,0] op_sel_hi:[1,0,0]\n"
"v_pk_fma_f32 v[116:117], v[116:117], v[16:17], s[60:61] op_sel:[0,1,0] op_sel_hi:[1,1,0]\n"
"v_pk_fma_f32 v[118:119], v[118:119], v[16:17], s[60:61] op_sel:[0,1,0] op_sel_hi:[1,1,0]\n"
"v_pk_fma_f32 v[76:77], v[112:113], s[62:63], s[62:63] op_sel:[0,0,1] op_sel_hi:[1,0,1]\n"
"v_pk_fma_f32 v[78:79], v[114:115], s[62:63], s[62:63] op_sel:[0,0,1] op_sel_hi:[1,0,1]\n"
"v_pk_fma_f32 v[80:81], v[116:117], s[62:63], s[62:63] op_sel:[0,0,1] op_sel_hi:[1,0,1]\n"
"v_pk_fma_f32 v[82:83], v[118:119], s[62:63], s[62:63] op_sel:[0,0,1] op_sel_hi:[1,0,1]\n"
"v_exp_f32_e64 v76, v76 clamp\n"
"v_exp_f32_e64 v77, v77 clamp\n"
"v_exp_f32_e64 v78, v78 clamp\n"
"v_exp_f32_e64 v79, v79 clamp\n"
"v_exp_f32_e64 v80, v80 clamp\n"
"v_exp_f32_e64 v81, v81 clamp\n"
"v_exp_f32_e64 v82, v82 clamp\n"
"v_exp_f32_e64 v83, v83 clamp\n"
"v_max_f32 v112, s60, v112\n"
"v_max_f32 v113, s60, v113\n"
"v_max_f32 v114, s60, v114\n"
"v_max_f32 v115, s60, v115\n"
"v_max_f32 v116, s60, v116\n"
"v_max_f32 v117, s60, v117\n"
"v_max_f32 v118, s60, v118\n"
"v_max_f32 v119, s60, v119\n"
"v_pk_add_f32 v[76:77], v[76:77], v[112:113]\n"
"v_pk_add_f32 v[78:79], v[78:79], v[114:115]\n"
"v_pk_add_f32 v[80:81], v[80:81], v[116:117]\n"
"v_pk_add_f32 v[82:83], v[82:83], v[118:119]\n"
"v_cvt_pk_bf16_f32 v28, v76, v77\n"
"v_cvt_pk_bf16_f32 v29, v78, v79\n"
"v_cvt_pk_bf16_f32 v30, v80, v81\n"
"v_cvt_pk_bf16_f32 v31, v82, v83\n"
"s_nop 0\n"
"v_permlane16_swap_b32 v28, v30\n"
"v_permlane16_swap_b32 v29, v31\n"
"s_nop 0\n"
"v_pk_fma_f32 v[120:121], v[120:121], v[18:19], s[60:61] op_sel:[0,0,0] op_sel_hi:[1,0,0]\n"
"v_pk_fma_f32 v[122:123], v[122:123], v[18:19], s[60:61] op_sel:[0,0,0] op_sel_hi:[1,0,0]\n"
"v_pk_fma_f32 v[124:125], v[124:125], v[18:19], s[60:61] op_sel:[0,1,0] op_sel_hi:[1,1,0]\n"
"v_pk_fma_f32 v[126:127], v[126:127], v[18:19], s[60:61] op_sel:[0,1,0] op_sel_hi:[1,1,0]\n"
"v_pk_fma_f32 v[76:77], v[120:121], s[62:63], s[62:63] op_sel:[0,0,1] op_sel_hi:[1,0,1]\n"
"v_pk_fma_f32 v[78:79], v[122:123], s[62:63], s[62:63] op_sel:[0,0,1] op_sel_hi:[1,0,1]\n"
"v_pk_fma_f32 v[80:81], v[124:125], s[62:63], s[62:63] op_sel:[0,0,1] op_sel_hi:[1,0,1]\n"
"v_pk_fma_f32 v[82:83], v[126:127], s[62:63], s[62:63] op_sel:[0,0,1] op_sel_hi:[1,0,1]\n"
"v_exp_f32_e64 v76, v76 clamp\n"
"v_exp_f32_e64 v77, v77 clamp\n"
"v_exp_f32_e64 v78, v78 clamp\n"
"v_exp_f32_e64 v79, v79 clamp\n"
"v_exp_f32_e64 v80, v80 clamp\n"
"v_exp_f32_e64 v81, v81 clamp\n"
"v_exp_f32_e64 v82, v82 clamp\n"
"v_exp_f32_e64 v83, v83 clamp\n"
"v_max_f32 v120, s60, v120\n"
"v_max_f32 v121, s60, v121\n"
"v_max_f32 v122, s60, v122\n"
"v_max_f32 v123, s60, v123\n"
"v_max_f32 v124, s60, v124\n"
"v_max_f32 v125, s60, v125\n"
"v_max_f32 v126, s60, v126\n"
"v_max_f32 v127, s60, v127\n"
"v_pk_add_f32 v[76:77], v[76:77], v[120:121]\n"
"v_pk_add_f32 v[78:79], v[78:79], v[122:123]\n"
"v_pk_add_f32 v[80:81], v[80:81], v[124:125]\n"
"v_pk_add_f32 v[82:83], v[82:83], v[126:127]\n"
"v_cvt_pk_bf16_f32 v32, v76, v77\n"
"v_cvt_pk_bf16_f32 v33, v78, v79\n"
"v_cvt_pk_bf16_f32 v34, v80, v81\n"
"v_cvt_pk_bf16_f32 v35, v82, v83\n"
"s_nop 0\n"
"v_permlane16_swap_b32 v32, v34\n"
"v_permlane16_swap_b32 v33, v35\n"
"s_nop 0\n"
"v_pk_fma_f32 v[128:129], v[128:129], v[20:21], s[60:61] op_sel:[0,0,0] op_sel_hi:[1,0,0]\n"
"v_pk_fma_f32 v[130:131], v[130:131], v[20:21], s[60:61] op_sel:[0,0,0] op_sel_hi:[1,0,0]\n"
"v_pk_fma_f32 v[132:133], v[132:133], v[20:21], s[60:61] op_sel:[0,1,0] op_sel_hi:[1,1,0]\n"
"v_pk_fma_f32 v[134:135], v[134:135], v[20:21], s[60:61] op_sel:[0,1,0] op_sel_hi:[1,1,0]\n"
"v_pk_fma_f32 v[76:77], v[128:129], s[62:63], s[62:63] op_sel:[0,0,1] op_sel_hi:[1,0,1]\n"
"v_pk_fma_f32 v[78:79], v[130:131], s[62:63], s[62:63] op_sel:[0,0,1] op_sel_hi:[1,0,1]\n"
"v_pk_fma_f32 v[80:81], v[132:133], s[62:63], s[62:63] op_sel:[0,0,1] op_sel_hi:[1,0,1]\n"
"v_pk_fma_f32 v[82:83], v[134:135], s[62:63], s[62:63] op_sel:[0,0,1] op_sel_hi:[1,0,1]\n"
"v_exp_f32_e64 v76, v76 clamp\n"
"v_exp_f32_e64 v77, v77 clamp\n"
"v_exp_f32_e64 v78, v78 clamp\n"
"v_exp_f32_e64 v79, v79 clamp\n"
"v_exp_f32_e64 v80, v80 clamp\n"
"v_exp_f32_e64 v81, v81 clamp\n"
"v_exp_f32_e64 v82, v82 clamp\n"
"v_exp_f32_e64 v83, v83 clamp\n"
"v_max_f32 v128, s60, v128\n"
"v_max_f32 v129, s60, v129\n"
"v_max_f32 v130, s60, v130\n"
"v_max_f32 v131, s60, v131\n"
"v_max_f32 v132, s60, v132\n"
"v_max_f32 v133, s60, v133\n"
"v_max_f32 v134, s60, v134\n"
"v_max_f32 v135, s60, v135\n"
"v_pk_add_f32 v[76:77], v[76:77], v[128:129]\n"
"v_pk_add_f32 v[78:79], v[78:79], v[130:131]\n"
"v_pk_add_f32 v[80:81], v[80:81], v[132:133]\n"
"v_pk_add_f32 v[82:83], v[82:83], v[134:135]\n"
"v_cvt_pk_bf16_f32 v36, v76, v77\n"
"v_cvt_pk_bf16_f32 v37, v78, v79\n"
"v_cvt_pk_bf16_f32 v38, v80, v81\n"
"v_cvt_pk_bf16_f32 v39, v82, v83\n"
"s_nop 0\n"
"v_permlane16_swap_b32 v36, v38\n"
"v_permlane16_swap_b32 v37, v39\n"
"s_mov_b64 exec, 0x7ff\n"
"s_waitcnt vmcnt(0)\n"
"v_cmp_ne_u64_e64 vcc, s[32:33], v[58:59]\n"
"s_nop 0\n"
"s_cbranch_vccz .Lfb_AD_26\n"
".Lfb_AP_25:\n"
"global_atomic_or_x2 v[58:59], v4, v[56:57], s[56:57] offset:0 sc0 sc1\n"
"s_waitcnt vmcnt(0)\n"
"v_cmp_ne_u64_e64 vcc, s[32:33], v[58:59]\n"
"s_nop 0\n"
"s_cbranch_vccz .Lfb_AD_26\n"
"s_sleep 0\n"
"s_sub_u32 s31, s31, 1\n"
"s_cbranch_scc0 .Lfb_AP_25\n"
"s_mov_b32 s31, 0\n"
".Lfb_AD_26:\n"
"s_mov_b64 exec, -1\n"
"s_mov_b32 exec_lo, 0x07ff07ff\n"
"s_mov_b32 exec_hi, 0x07ff07ff\n"
"global_load_dwordx4 v[52:55], v9, s[54:55] sc1\n"
"s_mov_b64 exec, -1\n"
"s_nop 0\n"
"global_load_dwordx4 v[160:163], v5, s[52:53] offset:0 sc1\n"
"global_load_dwordx4 v[164:167], v5, s[52:53] offset:1024 sc1\n"
"global_load_dwordx4 v[168:171], v5, s[52:53] offset:2048 sc1\n"
"global_load_dwordx4 v[172:175], v5, s[52:53] offset:3072 sc1\n"
"global_load_dwordx4 v[176:179], v8, s[52:53] offset:0 sc1\n"
"global_load_dwordx4 v[180:183], v8, s[52:53] offset:1024 sc1\n"
"global_load_dwordx4 v[184:187], v8, s[52:53] offset:2048 sc1\n"
"global_load_dwordx4 v[188:191], v8, s[52:53] offset:3072 sc1\n"
"global_load_dwordx4 v[192:195], v6, s[52:53] offset:0 sc1\n"
"global_load_dwordx4 v[196:199], v6, s[52:53] offset:1024 sc1\n"
"global_load_dwordx4 v[200:203], v6, s[52:53] offset:2048 sc1\n"
"v_mov_b32_e64 v7, 4\n"
"ds_write_b32 v70, v7 offset:16\n"
"v_add_u32 v61, 0x8000, v5\n"
"v_add_u32 v62, 0x10000, v5\n"
"v_add_u32 v63, 0x18000, v5\n"
"v_add_u32 v64, 0x20000, v5\n"
"v_add_u32 v65, 0x28000, v5\n"
"v_lshrrev_b32 v66, 3, v0\n"
"v_and_b32 v7, 7, v0\n"
"v_lshl_add_u32 v66, v66, 3, v7\n"
"v_lshlrev_b32 v66, 8, v66\n"
"v_mov_b32 v71, 5\n"
"v_mov_b32_e64 v7, 1\n"
"v_pk_fma_f32 v[136:137], v[136:137], v[22:23], s[60:61] op_sel:[0,0,0] op_sel_hi:[1,0,0]\n"
"v_pk_fma_f32 v[138:139], v[138:139], v[22:23], s[60:61] op_sel:[0,0,0] op_sel_hi:[1,0,0]\n"
"v_pk_fma_f32 v[140:141], v[140:141], v[22:23], s[60:61] op_sel:[0,1,0] op_sel_hi:[1,1,0]\n"
"v_pk_fma_f32 v[142:143], v[142:143], v[22:23], s[60:61] op_sel:[0,1,0] op_sel_hi:[1,1,0]\n"
"v_pk_fma_f32 v[76:77], v[136:137], s[62:63], s[62:63] op_sel:[0,0,1] op_sel_hi:[1,0,1]\n"
"v_pk_fma_f32 v[78:79], v[138:139], s[62:63], s[62:63] op_sel:[0,0,1] op_sel_hi:[1,0,1]\n"
"v_pk_fma_f32 v[80:81], v[140:141], s[62:63], s[62:63] op_sel:[0,0,1] op_sel_hi:[1,0,1]\n"
"v_pk_fma_f32 v[82:83], v[142:143], s[62:63], s[62:63] op_sel:[0,0,1] op_sel_hi:[1,0,1]\n"
"v_exp_f32_e64 v76, v76 clamp\n"
"v_exp_f32_e64 v77, v77 clamp\n"
"v_exp_f32_e64 v78, v78 clamp\n"
"v_exp_f32_e64 v79, v79 clamp\n"
"v_exp_f32_e64 v80, v80 clamp\n"
"v_exp_f32_e64 v81, v81 clamp\n"
"v_exp_f32_e64 v82, v82 clamp\n"
"v_exp_f32_e64 v83, v83 clamp\n"
"v_max_f32 v136, s60, v136\n"
"v_max_f32 v137, s60, v137\n"
"v_max_f32 v138, s60, v138\n"
"v_max_f32 v139, s60, v139\n"
"v_max_f32 v140, s60, v140\n"
"v_max_f32 v141, s60, v141\n"
"v_max_f32 v142, s60, v142\n"
"v_max_f32 v143, s60, v143\n"
"v_pk_add_f32 v[76:77], v[76:77], v[136:137]\n"
"v_pk_add_f32 v[78:79], v[78:79], v[138:139]\n"
"v_pk_add_f32 v[80:81], v[80:81], v[140:141]\n"
"v_pk_add_f32 v[82:83], v[82:83], v[142:143]\n"
"v_cvt_pk_bf16_f32 v40, v76, v77\n"
"v_cvt_pk_bf16_f32 v41, v78, v79\n"
"v_cvt_pk_bf16_f32 v42, v80, v81\n"
"v_cvt_pk_bf16_f32 v43, v82, v83\n"
"s_nop 0\n"
"v_permlane16_swap_b32 v40, v42\n"
"v_permlane16_swap_b32 v41, v43\n"
"s_nop 0\n"
"v_pk_fma_f32 v[144:145], v[144:145], v[24:25], s[60:61] op_sel:[0,0,0] op_sel_hi:[1,0,0]\n"
"v_pk_fma_f32 v[146:147], v[146:147], v[24:25], s[60:61] op_sel:[0,0,0] op_sel_hi:[1,0,0]\n"
"v_pk_fma_f32 v[148:149], v[148:149], v[24:25], s[60:61] op_sel:[0,1,0] op_sel_hi:[1,1,0]\n"
"v_pk_fma_f32 v[150:151], v[150:151], v[24:25], s[60:61] op_sel:[0,1,0] op_sel_hi:[1,1,0]\n"
"v_pk_fma_f32 v[76:77], v[144:145], s[62:63], s[62:63] op_sel:[0,0,1] op_sel_hi:[1,0,1]\n"
"v_pk_fma_f32 v[78:79], v[146:147], s[62:63], s[62:63] op_sel:[0,0,1] op_sel_hi:[1,0,1]\n"
"v_pk_fma_f32 v[80:81], v[148:149], s[62:63], s[62:63] op_sel:[0,0,1] op_sel_hi:[1,0,1]\n"
"v_pk_fma_f32 v[82:83], v[150:151], s[62:63], s[62:63] op_sel:[0,0,1] op_sel_hi:[1,0,1]\n"
"v_exp_f32_e64 v76, v76 clamp\n"
"v_exp_f32_e64 v77, v77 clamp\n"
"v_exp_f32_e64 v78, v78 clamp\n"
"v_exp_f32_e64 v79, v79 clamp\n"
"v_exp_f32_e64 v80, v80 clamp\n"
"v_exp_f32_e64 v81, v81 clamp\n"
"v_exp_f32_e64 v82, v82 clamp\n"
"v_exp_f32_e64 v83, v83 clamp\n"
"v_max_f32 v144, s60, v144\n"
"v_max_f32 v145, s60, v145\n"
"v_max_f32 v146, s60, v146\n"
"v_max_f32 v147, s60, v147\n"
"v_max_f32 v148, s60, v148\n"
"v_max_f32 v149, s60, v149\n"
"v_max_f32 v150, s60, v150\n"
"v_max_f32 v151, s60, v151\n"
"v_pk_add_f32 v[76:77], v[76:77], v[144:145]\n"
"v_pk_add_f32 v[78:79], v[78:79], v[146:147]\n"
"v_pk_add_f32 v[80:81], v[80:81], v[148:149]\n"
"v_pk_add_f32 v[82:83], v[82:83], v[150:151]\n"
"v_cvt_pk_bf16_f32 v44, v76, v77\n"
"v_cvt_pk_bf16_f32 v45, v78, v79\n"
"v_cvt_pk_bf16_f32 v46, v80, v81\n"
"v_cvt_pk_bf16_f32 v47, v82, v83\n"
"s_nop 0\n"
"v_permlane16_swap_b32 v44, v46\n"
"v_permlane16_swap_b32 v45, v47\n"
"s_nop 0\n"
"v_pk_fma_f32 v[152:153], v[152:153], v[26:27], s[60:61] op_sel:[0,0,0] op_sel_hi:[1,0,0]\n"
"v_pk_fma_f32 v[154:155], v[154:155], v[26:27], s[60:61] op_sel:[0,0,0] op_sel_hi:[1,0,0]\n"
"v_pk_fma_f32 v[156:157], v[156:157], v[26:27], s[60:61] op_sel:[0,1,0] op_sel_hi:[1,1,0]\n"
"v_pk_fma_f32 v[158:159], v[158:159], v[26:27], s[60:61] op_sel:[0,1,0] op_sel_hi:[1,1,0]\n"
"v_pk_fma_f32 v[76:77], v[152:153], s[62:63], s[62:63] op_sel:[0,0,1] op_sel_hi:[1,0,1]\n"
"v_pk_fma_f32 v[78:79], v[154:155], s[62:63], s[62:63] op_sel:[0,0,1] op_sel_hi:[1,0,1]\n"
"v_pk_fma_f32 v[80:81], v[156:157], s[62:63], s[62:63] op_sel:[0,0,1] op_sel_hi:[1,0,1]\n"
"v_pk_fma_f32 v[82:83], v[158:159], s[62:63], s[62:63] op_sel:[0,0,1] op_sel_hi:[1,0,1]\n"
"v_exp_f32_e64 v76, v76 clamp\n"
"v_exp_f32_e64 v77, v77 clamp\n"
"v_exp_f32_e64 v78, v78 clamp\n"
"v_exp_f32_e64 v79, v79 clamp\n"
"v_exp_f32_e64 v80, v80 clamp\n"
"v_exp_f32_e64 v81, v81 clamp\n"
"v_exp_f32_e64 v82, v82 clamp\n"
"v_exp_f32_e64 v83, v83 clamp\n"
"v_max_f32 v152, s60, v152\n"
"v_max_f32 v153, s60, v153\n"
"v_max_f32 v154, s60, v154\n"
"v_max_f32 v155, s60, v155\n"
"v_max_f32 v156, s60, v156\n"
"v_max_f32 v157, s60, v157\n"
"v_max_f32 v158, s60, v158\n"
"v_max_f32 v159, s60, v159\n"
"v_pk_add_f32 v[76:77], v[76:77], v[152:153]\n"
"v_pk_add_f32 v[78:79], v[78:79], v[154:155]\n"
"v_pk_add_f32 v[80:81], v[80:81], v[156:157]\n"
"v_pk_add_f32 v[82:83], v[82:83], v[158:159]\n"
"v_cvt_pk_bf16_f32 v48, v76, v77\n"
"v_cvt_pk_bf16_f32 v49, v78, v79\n"
"v_cvt_pk_bf16_f32 v50, v80, v81\n"
"v_cvt_pk_bf16_f32 v51, v82, v83\n"
"s_nop 0\n"
"v_permlane16_swap_b32 v48, v50\n"
"v_permlane16_swap_b32 v49, v51\n"
".Lfb_EQ_GS:\n"
"s_waitcnt vmcnt(11)\n"
"v_xor_b32 v52, s70, v52\n"
"v_xor_b32 v53, s71, v53\n"
"v_xor_b32 v54, s72, v54\n"
"v_xor_b32 v55, s73, v55\n"
"v_or3_b32 v15, v52, v53, 0\n"
"v_or3_b32 v15, v54, v55, v15\n"
"v_add_f32_dpp v52, v52, v52 quad_perm:[1,0,3,2] row_mask:0xf bank_mask:0xf\n"
"v_add_f32_dpp v53, v53, v53 quad_perm:[1,0,3,2] row_mask:0xf bank_mask:0xf\n"
"v_add_f32_dpp v54, v54, v54 quad_perm:[1,0,3,2] row_mask:0xf bank_mask:0xf\n"
"v_add_f32_dpp v55, v55, v55 quad_perm:[1,0,3,2] row_mask:0xf bank_mask:0xf\n"
"v_add_f32_dpp v52, v52, v52 quad_perm:[2,3,0,1] row_mask:0xf bank_mask:0xf\n"
"v_add_f32_dpp v53, v53, v53 quad_perm:[2,3,0,1] row_mask:0xf bank_mask:0xf\n"
"v_add_f32_dpp v54, v54, v54 quad_perm:[2,3,0,1] row_mask:0xf bank_mask:0xf\n"
"v_add_f32_dpp v55, v55, v55 quad_perm:[2,3,0,1] row_mask:0xf bank_mask:0xf\n"
"v_add_f32_dpp v52, v52, v52 row_half_mirror row_mask:0xf bank_mask:0xf\n"
"v_add_f32_dpp v53, v53, v53 row_half_mirror row_mask:0xf bank_mask:0xf\n"
"v_add_f32_dpp v54, v54, v54 row_half_mirror row_mask:0xf bank_mask:0xf\n"
"v_add_f32_dpp v55, v55, v55 row_half_mirror row_mask:0xf bank_mask:0xf\n"
"v_add_f32_dpp v52, v52, v52 row_mirror row_mask:0xf bank_mask:0xf\n"
"v_add_f32_dpp v53, v53, v53 row_mirror row_mask:0xf bank_mask:0xf\n"
"v_add_f32_dpp v54, v54, v54 row_mirror row_mask:0xf bank_mask:0xf\n"
"v_add_f32_dpp v55, v55, v55 row_mirror row_mask:0xf bank_mask:0xf\n"
"v_cvt_pk_bf16_f32 v86, v52, v53\n"
"v_cvt_pk_bf16_f32 v87, v54, v55\n"
"s_waitcnt vmcnt(10)\n"
"v_xor_b32 v160, s70, v160\n"
"v_xor_b32_e64 v161, s71, v161\n"
"v_or3_b32 v15, v160, v161, v15\n"
"s_waitcnt vmcnt(9)\n"
"v_xor_b32 v164, s70, v164\n"
"v_xor_b32_e64 v165, s71, v165\n"
"v_or3_b32 v15, v164, v165, v15\n"
"v_pk_add_f32 v[160:161], v[160:161], v[164:165]\n"
"v_pk_add_f32 v[162:163], v[162:163], v[166:167]\n"
"s_waitcnt vmcnt(8)\n"
"v_xor_b32 v168, s70, v168\n"
"v_xor_b32_e64 v169, s71, v169\n"
"v_or3_b32 v15, v168, v169, v15\n"
"v_pk_add_f32 v[160:161], v[160:161], v[168:169]\n"
"v_pk_add_f32 v[162:163], v[162:163], v[170:171]\n"
"s_waitcnt vmcnt(7)\n"
"v_xor_b32 v172, s70, v172\n"
"v_xor_b32_e64 v173, s71, v173\n"
"v_or3_b32 v15, v172, v173, v15\n"
"v_pk_add_f32 v[160:161], v[160:161], v[172:173]\n"
"v_pk_add_f32 v[162:163], v[162:163], v[174:175]\n"
"s_waitcnt vmcnt(6)\n"
"v_xor_b32 v176, s70, v176\n"
"v_xor_b32_e64 v177, s71, v177\n"
"v_or3_b32 v15, v176, v177, v15\n"
"v_pk_add_f32 v[160:161], v[160:161], v[176:177]\n"
"v_pk_add_f32 v[162:163], v[162:163], v[178:179]\n"
"s_waitcnt vmcnt(5)\n"
"v_xor_b32 v180, s70, v180\n"
"v_xor_b32_e64 v181, s71, v181\n"
"v_or3_b32 v15, v180, v181, v15\n"
"v_pk_add_f32 v[160:161], v[160:161], v[180:181]\n"
"v_pk_add_f32 v[162:163], v[162:163], v[182:183]\n"
"s_waitcnt vmcnt(4)\n"
"v_xor_b32 v184, s70, v184\n"
"v_xor_b32_e64 v185, s71, v185\n"
"v_or3_b32 v15, v184, v185, v15\n"
"v_pk_add_f32 v[160:161], v[160:161], v[184:185]\n"
"v_pk_add_f32 v[162:163], v[162:163], v[186:187]\n"
"s_waitcnt vmcnt(3)\n"
"v_xor_b32 v188, s70, v188\n"
"v_xor_b32_e64 v189, s71, v189\n"
"v_or3_b32 v15, v188, v189, v15\n"
"v_pk_add_f32 v[160:161], v[160:161], v[188:189]\n"
"v_pk_add_f32 v[162:163], v[162:163], v[190:191]\n"
"s_waitcnt vmcnt(2)\n"
"v_xor_b32 v192, s70, v192\n"
"v_xor_b32_e64 v193, s71, v193\n"
"v_or3_b32 v15, v192, v193, v15\n"
"v_pk_add_f32 v[160:161], v[160:161], v[192:193]\n"
"v_pk_add_f32 v[162:163], v[162:163], v[194:195]\n"
"s_waitcnt vmcnt(1)\n"
"v_xor_b32 v196, s70, v196\n"
"v_xor_b32_e64 v197, s71, v197\n"
"v_or3_b32 v15, v196, v197, v15\n"
"v_pk_add_f32 v[160:161], v[160:161], v[196:197]\n"
"v_pk_add_f32 v[162:163], v[162:163], v[198:199]\n"
"s_waitcnt vmcnt(0)\n"
"v_xor_b32 v200, s70, v200\n"
"v_xor_b32_e64 v201, s71, v201\n"
"v_or3_b32 v15, v200, v201, v15\n"
"v_and_b32 v15, 0xff, v15\n"
"v_cmp_ne_u32_e64 vcc, 0, v15\n"
"v_pk_add_f32 v[160:161], v[160:161], v[200:201]\n"
"v_pk_add_f32 v[162:163], v[162:163], v[202:203]\n"
"s_cbranch_vccnz .Lfb_EQ_GR\n"
".Lfb_EQ_GD:\n"
"s_nop 0\n"
"v_cvt_pk_bf16_f32 v84, v160, v161\n"
"v_cvt_pk_bf16_f32 v85, v162, v163\n"
"s_nop 0\n"
"v_permlane16_swap_b32 v84, v86\n"
"v_permlane16_swap_b32 v85, v87\n"
"s_nop 1\n"
"v_mfma_f32_32x32x16_bf16 v[112:127], v[84:87], v[28:31], 0\n"
"v_mfma_f32_32x32x16_bf16 v[128:143], v[84:87], v[32:35], 0\n"
"s_nop 10\n"
"v_add_f32 v120, s60, v120\n"
"v_rcp_f32 v120, v120\n"
"s_nop 0\n"
"v_pk_mul_f32 v[112:113], v[112:113], v[120:121] op_sel_hi:[1,0]\n"
"v_pk_mul_f32 v[114:115], v[114:115], v[120:121] op_sel_hi:[1,0]\n"
"v_pk_mul_f32 v[116:117], v[116:117], v[120:121] op_sel_hi:[1,0]\n"
"v_pk_mul_f32 v[118:119], v[118:119], v[120:121] op_sel_hi:[1,0]\n"
"v_mfma_f32_32x32x16_bf16 v[144:159], v[84:87], v[36:39], 0\n"
"v_cvt_pk_bf16_f32 v124, v112, v113\n"
"v_cvt_pk_bf16_f32 v125, v114, v115\n"
"v_cvt_pk_bf16_f32 v126, v116, v117\n"
"v_cvt_pk_bf16_f32 v127, v118, v119\n"
"global_store_dwordx4 v5, v[124:127], s[68:69] sc1\n"
"v_add_f32 v136, s60, v136\n"
"v_rcp_f32_e64 v136, v136\n"
"s_nop 0\n"
"v_pk_mul_f32 v[128:129], v[128:129], v[136:137] op_sel_hi:[1,0]\n"
"v_pk_mul_f32 v[130:131], v[130:131], v[136:137] op_sel_hi:[1,0]\n"
"v_pk_mul_f32 v[132:133], v[132:133], v[136:137] op_sel_hi:[1,0]\n"
"v_pk_mul_f32 v[134:135], v[134:135], v[136:137] op_sel_hi:[1,0]\n"
"v_mfma_f32_32x32x16_bf16 v[208:223], v[84:87], v[40:43], 0\n"
"v_cvt_pk_bf16_f32 v140, v128, v129\n"
"v_cvt_pk_bf16_f32 v141, v130, v131\n"
"v_cvt_pk_bf16_f32 v142, v132, v133\n"
"v_cvt_pk_bf16_f32 v143, v134, v135\n"
"global_store_dwordx4 v61, v[140:143], s[68:69] sc1\n"
"v_add_f32 v152, s60, v152\n"
"v_rcp_f32_e64 v152, v152\n"
"s_nop 0\n"
"v_pk_mul_f32 v[144:145], v[144:145], v[152:153] op_sel_hi:[1,0]\n"
"v_pk_mul_f32 v[146:147], v[146:147], v[152:153] op_sel_hi:[1,0]\n"
"v_pk_mul_f32 v[148:149], v[148:149], v[152:153] op_sel_hi:[1,0]\n"
"v_pk_mul_f32 v[150:151], v[150:151], v[152:153] op_sel_hi:[1,0]\n"
"v_mfma_f32_32x32x16_bf16 v[224:239], v[84:87], v[44:47], 0\n"
"v_cvt_pk_bf16_f32 v156, v144, v145\n"
"v_cvt_pk_bf16_f32 v157, v146, v147\n"
"v_cvt_pk_bf16_f32 v158, v148, v149\n"
"v_cvt_pk_bf16_f32 v159, v150, v151\n"
"global_store_dwordx4 v62, v[156:159], s[68:69] sc1\n"
"v_add_f32 v216, s60, v216\n"
"v_rcp_f32_e64 v216, v216\n"
"s_nop 0\n"
"v_pk_mul_f32 v[208:209], v[208:209], v[216:217] op_sel_hi:[1,0]\n"
"v_pk_mul_f32 v[210:211], v[210:211], v[216:217] op_sel_hi:[1,0]\n"
"v_pk_mul_f32 v[212:213], v[212:213], v[216:217] op_sel_hi:[1,0]\n"
"v_pk_mul_f32 v[214:215], v[214:215], v[216:217] op_sel_hi:[1,0]\n"
"v_mfma_f32_32x32x16_bf16 v[240:255], v[84:87], v[48:51], 0\n"
"v_cvt_pk_bf16_f32 v220, v208, v209\n"
"v_cvt_pk_bf16_f32 v221, v210, v211\n"
"v_cvt_pk_bf16_f32 v222, v212, v213\n"
"v_cvt_pk_bf16_f32 v223, v214, v215\n"
"global_store_dwordx4 v63, v[220:223], s[68:69] sc1\n"
"s_cmp_eq_u32 s21, 10\n"
"s_cbranch_scc1 .Lfb_EQ_OD\n"
"v_add_f32 v232, s60, v232\n"
"v_rcp_f32_e64 v232, v232\n"
"s_nop 0\n"
"v_pk_mul_f32 v[224:225], v[224:225], v[232:233] op_sel_hi:[1,0]\n"
"v_pk_mul_f32 v[226:227], v[226:227], v[232:233] op_sel_hi:[1,0]\n"
"v_pk_mul_f32 v[228:229], v[228:229], v[232:233] op_sel_hi:[1,0]\n"
"v_pk_mul_f32 v[230:231], v[230:231], v[232:233] op_sel_hi:[1,0]\n"
"v_cvt_pk_bf16_f32 v236, v224, v225\n"
"v_cvt_pk_bf16_f32 v237, v226, v227\n"
"v_cvt_pk_bf16_f32 v238, v228, v229\n"
"v_cvt_pk_bf16_f32 v239, v230, v231\n"
"global_store_dwordx4 v64, v[236:239], s[68:69] sc1\n"
"v_add_f32 v248, s60, v248\n"
"v_rcp_f32_e64 v248, v248\n"
"s_nop 0\n"
"v_pk_mul_f32 v[240:241], v[240:241], v[248:249] op_sel_hi:[1,0]\n"
"v_pk_mul_f32 v[242:243], v[242:243], v[248:249] op_sel_hi:[1,0]\n"
"v_pk_mul_f32 v[244:245], v[244:245], v[248:249] op_sel_hi:[1,0]\n"
"v_pk_mul_f32 v[246:247], v[246:247], v[248:249] op_sel_hi:[1,0]\n"
"v_cvt_pk_bf16_f32 v252, v240, v241\n"
"v_cvt_pk_bf16_f32 v253, v242, v243\n"
"v_cvt_pk_bf16_f32 v254, v244, v245\n"
"v_cvt_pk_bf16_f32 v255, v246, v247\n"
"global_store_dwordx4 v65, v[252:255], s[68:69] sc1\n"
".Lfb_EQ_OD:\n"
"ds_write_b32 v70, v71 offset:16\n"
"s_waitcnt vmcnt(0)\n"
"s_mov_b64 exec, s[64:65]\n"
"global_atomic_swap_x2 v66, v[10:11], s[74:75] offset:0 sc1\n"
"s_mov_b64 exec, -1\n"
".Lfb_C_P2:\n"
"v_lshrrev_b32 v2, 4, v0\n"
"v_and_b32 v1, 7, v0\n"
"v_xor_b32 v1, v1, v2\n"
"v_lshlrev_b32 v1, 4, v1\n"
"v_lshrrev_b32 v3, 3, v0\n"
"v_lshl_add_u32 v3, v3, 10, v1\n"
"v_xor_b32 v4, 0x2040, v3\n"
"s_lshl_b32 s58, s20, 11\n"
"s_add_u32 s58, s58, 65536\n"
"v_bfe_u32 v1, v0, 1, 3\n"
"v_lshrrev_b32 v5, 5, v0\n"
"v_xor_b32 v1, v1, v5\n"
"v_and_b32 v5, 1, v2\n"
"v_lshl_add_u32 v1, v1, 1, v5\n"
"v_and_b32_e64 v5, 15, v0\n"
"v_lshl_add_u32 v1, v5, 4, v1\n"
"v_lshl_add_u32 v172, v1, 3, s58\n"
"v_xor_b32 v173, 32, v172\n"
"v_xor_b32 v174, 64, v172\n"
"v_xor_b32 v175, 96, v172\n"
"v_lshl_add_u32 v5, v0, 4, s58\n"
"s_lshl2_add_u32 s34, s29, s20\n"
"s_lshl_b32 s34, s34, 14\n"
"s_lshl_b32 s35, s30, 7\n"
"s_add_u32 s35, s35, s34\n"
"s_add_u32 s54, s16, s35\n"
"s_addc_u32 s55, s17, 0\n"
"s_add_u32 s68, s4, s35\n"
"s_addc_u32 s69, s5, 0\n"
"s_mul_i32 s34, s20, 0x4100\n"
"s_and_b32 s34, s34, 0x8100\n"
"s_lshl_b32 s35, s29, 16\n"
"s_add_u32 s34, s34, s35\n"
"s_add_u32 s34, s34, 7344128\n"
"s_add_u32 s52, s18, s34\n"
"s_addc_u32 s53, s19, 0\n"
"s_lshr_b32 s35, s30, 0\n"
"s_lshl3_add_u32 s34, s29, s35\n"
"s_lshl_b32 s34, s34, 8\n"
"s_add_u32 s34, s34, 9961472\n"
"s_add_u32 s56, s18, s34\n"
"s_addc_u32 s57, s19, 0\n"
"v_lshrrev_b32 v8, 2, v0\n"
"v_bfe_u32 v9, v0, 3, 2\n"
"v_and_b32 v10, 3, v0\n"
"v_lshlrev_b32 v18, 7, v8\n"
"v_lshl_add_u32 v18, v9, 5, v18\n"
"v_lshl_add_u32 v18, v10, 3, v18\n"
"v_xor_b32 v19, 32, v18\n"
"v_xor_b32 v20, 64, v18\n"
"v_xor_b32 v21, 64, v19\n"
"v_lshlrev_b32 v6, 2, v10\n"
"v_add_u32 v6, 161824, v6\n"
"v_lshlrev_b32 v7, 3, v0\n"
"v_mov_b64 v[16:17], 0\n"
"v_lshlrev_b32_e64 v15, 4, v0\n"
"v_and_b32 v8, 0xf0, v15\n"
"v_lshl_add_u32 v8, v2, 9, v8\n"
"v_add_u32 v9, 0x2000, v8\n"
"v_add_u32 v10, 0x4000, v8\n"
"v_add_u32 v11, 0x6000, v8\n"
"s_add_u32 s34, s20, 3\n"
"s_and_b32 s34, s34, 3\n"
"s_mul_i32 s34, s34, 12288\n"
"s_add_u32 s34, s34, 98304\n"
"v_add_u32_e64 v12, s34, v15\n"
"s_lshl2_add_u32 s34, s20, 161824\n"
"v_mov_b32_e64 v13, s34\n"
".Lfb_LP_28:\n"
"ds_read_b32 v14, v6 offset:0\n"
"s_waitcnt lgkmcnt(0)\n"
"v_cmp_gt_u32 vcc, 1, v14\n"
"s_nop 0\n"
"s_cbranch_vccz .Lfb_LD_29\n"
"s_sleep 2\n"
"s_sub_u32 s31, s31, 1\n"
"s_cbranch_scc0 .Lfb_LP_28\n"
"s_mov_b32 s31, 0\n"
".Lfb_LD_29:\n"
".Lfb_C_WD:\n"
"s_add_u32 m0, s58, 0\n"
"s_nop 0\n"
"global_load_lds_dwordx4 v3, s[68:69]\n"
"s_add_u32 m0, s58, 1024\n"
"s_nop 0\n"
"s_nop 0\n"
"global_load_lds_dwordx4 v4, s[68:69]\n"
"ds_read_b64_tr_b16 v[24:25], v18 offset:0\n"
"ds_read_b64_tr_b16 v[26:27], v18 offset:2048\n"
"ds_read_b64_tr_b16 v[28:29], v19 offset:0\n"
"ds_read_b64_tr_b16 v[30:31], v19 offset:2048\n"
"ds_read_b64_tr_b16 v[32:33], v20 offset:0\n"
"ds_read_b64_tr_b16 v[34:35], v20 offset:2048\n"
"ds_read_b64_tr_b16 v[36:37], v21 offset:0\n"
"ds_read_b64_tr_b16 v[38:39], v21 offset:2048\n"
"ds_read_b64_tr_b16 v[40:41], v18 offset:4096\n"
"ds_read_b64_tr_b16 v[42:43], v18 offset:6144\n"
"ds_read_b64_tr_b16 v[44:45], v19 offset:4096\n"
"ds_read_b64_tr_b16 v[46:47], v19 offset:6144\n"
"ds_read_b64_tr_b16 v[48:49], v20 offset:4096\n"
"ds_read_b64_tr_b16 v[50:51], v20 offset:6144\n"
"ds_read_b64_tr_b16 v[52:53], v21 offset:4096\n"
"ds_read_b64_tr_b16 v[54:55], v21 offset:6144\n"
"ds_read_b64_tr_b16 v[56:57], v18 offset:8192\n"
"ds_read_b64_tr_b16 v[58:59], v18 offset:10240\n"
"ds_read_b64_tr_b16 v[60:61], v19 offset:8192\n"
"ds_read_b64_tr_b16 v[62:63], v19 offset:10240\n"
"ds_read_b64_tr_b16 v[64:65], v20 offset:8192\n"
"ds_read_b64_tr_b16 v[66:67], v20 offset:10240\n"
"ds_read_b64_tr_b16 v[68:69], v21 offset:8192\n"
"ds_read_b64_tr_b16 v[70:71], v21 offset:10240\n"
"ds_read_b64_tr_b16 v[72:73], v18 offset:12288\n"
"ds_read_b64_tr_b16 v[74:75], v18 offset:14336\n"
"ds_read_b64_tr_b16 v[76:77], v19 offset:12288\n"
"ds_read_b64_tr_b16 v[78:79], v19 offset:14336\n"
"ds_read_b64_tr_b16 v[80:81], v20 offset:12288\n"
"ds_read_b64_tr_b16 v[82:83], v20 offset:14336\n"
"ds_read_b64_tr_b16 v[84:85], v21 offset:12288\n"
"ds_read_b64_tr_b16 v[86:87], v21 offset:14336\n"
"ds_read_b64_tr_b16 v[88:89], v18 offset:16384\n"
"ds_read_b64_tr_b16 v[90:91], v18 offset:18432\n"
"ds_read_b64_tr_b16 v[92:93], v19 offset:16384\n"
"ds_read_b64_tr_b16 v[94:95], v19 offset:18432\n"
"ds_read_b64_tr_b16 v[96:97], v20 offset:16384\n"
"ds_read_b64_tr_b16 v[98:99], v20 offset:18432\n"
"ds_read_b64_tr_b16 v[100:101], v21 offset:16384\n"
"ds_read_b64_tr_b16 v[102:103], v21 offset:18432\n"
"ds_read_b64_tr_b16 v[104:105], v18 offset:20480\n"
"ds_read_b64_tr_b16 v[106:107], v18 offset:22528\n"
"ds_read_b64_tr_b16 v[108:109], v19 offset:20480\n"
"ds_read_b64_tr_b16 v[110:111], v19 offset:22528\n"
"ds_read_b64_tr_b16 v[112:113], v20 offset:20480\n"
"ds_read_b64_tr_b16 v[114:115], v20 offset:22528\n"
"ds_read_b64_tr_b16 v[116:117], v21 offset:20480\n"
"ds_read_b64_tr_b16 v[118:119], v21 offset:22528\n"
"ds_read_b64_tr_b16 v[120:121], v18 offset:24576\n"
"ds_read_b64_tr_b16 v[122:123], v18 offset:26624\n"
"ds_read_b64_tr_b16 v[124:125], v19 offset:24576\n"
"ds_read_b64_tr_b16 v[126:127], v19 offset:26624\n"
"ds_read_b64_tr_b16 v[128:129], v20 offset:24576\n"
"ds_read_b64_tr_b16 v[130:131], v20 offset:26624\n"
"ds_read_b64_tr_b16 v[132:133], v21 offset:24576\n"
"ds_read_b64_tr_b16 v[134:135], v21 offset:26624\n"
"ds_read_b64_tr_b16 v[136:137], v18 offset:28672\n"
"ds_read_b64_tr_b16 v[138:139], v18 offset:30720\n"
"ds_read_b64_tr_b16 v[140:141], v19 offset:28672\n"
"ds_read_b64_tr_b16 v[142:143], v19 offset:30720\n"
"ds_read_b64_tr_b16 v[144:145], v20 offset:28672\n"
"ds_read_b64_tr_b16 v[146:147], v20 offset:30720\n"
"ds_read_b64_tr_b16 v[148:149], v21 offset:28672\n"
"ds_read_b64_tr_b16 v[150:151], v21 offset:30720\n"
"s_waitcnt vmcnt(0)\n"
"s_nop 0\n"
"ds_read_b64 v[240:241], v172\n"
"ds_read_b64 v[242:243], v173\n"
"ds_read_b64 v[244:245], v174\n"
"ds_read_b64 v[246:247], v175\n"
"s_waitcnt lgkmcnt(0)\n"
"v_lshlrev_b32 v152, 16, v240\n"
"v_and_b32 v153, 0xffff0000, v240\n"
"v_lshlrev_b32_e64 v154, 16, v241\n"
"v_and_b32 v155, 0xffff0000, v241\n"
"v_lshlrev_b32_e64 v156, 16, v242\n"
"v_and_b32 v157, 0xffff0000, v242\n"
"v_lshlrev_b32_e64 v158, 16, v243\n"
"v_and_b32 v159, 0xffff0000, v243\n"
"v_lshlrev_b32_e64 v160, 16, v244\n"
"v_and_b32 v161, 0xffff0000, v244\n"
"v_lshlrev_b32_e64 v162, 16, v245\n"
"v_and_b32 v163, 0xffff0000, v245\n"
"v_lshlrev_b32_e64 v164, 16, v246\n"
"v_and_b32 v165, 0xffff0000, v246\n"
"v_lshlrev_b32_e64 v166, 16, v247\n"
"v_and_b32 v167, 0xffff0000, v247\n"
".Lfb_LP_30:\n"
"ds_read_b32 v14, v6 offset:0\n"
"s_waitcnt lgkmcnt(0)\n"
"v_cmp_gt_u32 vcc, 2, v14\n"
"s_nop 0\n"
"s_cbranch_vccz .Lfb_LD_31\n"
"s_sleep 2\n"
"s_sub_u32 s31, s31, 1\n"
"s_cbranch_scc0 .Lfb_LP_30\n"
"s_mov_b32 s31, 0\n"
".Lfb_LD_31:\n"
"s_bfm_b64 s[38:39], 32, 0\n"
"s_mov_b64 exec, s[38:39]\n"
".Lfb_AP_32:\n"
"global_atomic_or_x2 v[14:15], v7, v[16:17], s[56:57] offset:0 sc0 sc1\n"
"s_waitcnt vmcnt(0)\n"
"v_cmp_ne_u64_e64 vcc, s[32:33], v[14:15]\n"
"s_nop 0\n"
"s_cbranch_vccz .Lfb_AD_33\n"
"s_sleep 0\n"
"s_sub_u32 s31, s31, 1\n"
"s_cbranch_scc0 .Lfb_AP_32\n"
"s_mov_b32 s31, 0\n"
".Lfb_AD_33:\n"
"s_mov_b64 exec, -1\n"
".Lfb_LP_35:\n"
"ds_read_b32 v14, v6 offset:0\n"
"s_waitcnt lgkmcnt(0)\n"
"v_cmp_gt_u32 vcc, 3, v14\n"
"s_nop 0\n"
"s_cbranch_vccz .Lfb_LD_36\n"
"s_sleep 1\n"
"s_sub_u32 s31, s31, 1\n"
"s_cbranch_scc0 .Lfb_LP_35\n"
"s_mov_b32 s31, 0\n"
".Lfb_LD_36:\n"
"global_load_dwordx4 v[192:195], v8, s[52:53] offset:-4096 sc1\n"
"global_load_dwordx4 v[196:199], v8, s[52:53] offset:-2048 sc1\n"
"global_load_dwordx4 v[200:203], v8, s[52:53] offset:0 sc1\n"
"global_load_dwordx4 v[204:207], v8, s[52:53] offset:2048 sc1\n"
"global_load_dwordx4 v[208:211], v9, s[52:53] offset:-4096 sc1\n"
"global_load_dwordx4 v[212:215], v9, s[52:53] offset:-2048 sc1\n"
"global_load_dwordx4 v[216:219], v9, s[52:53] offset:0 sc1\n"
"global_load_dwordx4 v[220:223], v9, s[52:53] offset:2048 sc1\n"
"global_load_dwordx4 v[224:227], v10, s[52:53] offset:-4096 sc1\n"
"global_load_dwordx4 v[228:231], v10, s[52:53] offset:-2048 sc1\n"
"global_load_dwordx4 v[232:235], v10, s[52:53] offset:0 sc1\n"
"global_load_dwordx4 v[236:239], v10, s[52:53] offset:2048 sc1\n"
"global_load_dwordx4 v[240:243], v11, s[52:53] offset:-4096 sc1\n"
"global_load_dwordx4 v[244:247], v11, s[52:53] offset:-2048 sc1\n"
"global_load_dwordx4 v[248:251], v11, s[52:53] offset:0 sc1\n"
"global_load_dwordx4 v[252:255], v11, s[52:53] offset:2048 sc1\n"
".Lfb_LP_37:\n"
"ds_read_b32 v14, v6 offset:0\n"
"s_waitcnt lgkmcnt(0)\n"
"v_cmp_gt_u32_e64 vcc, 4, v14\n"
"s_nop 0\n"
"s_cbranch_vccz .Lfb_LD_38\n"
"s_sleep 1\n"
"s_sub_u32 s31, s31, 1\n"
"s_cbranch_scc0 .Lfb_LP_37\n"
"s_mov_b32 s31, 0\n"
".Lfb_LD_38:\n"
"s_waitcnt vmcnt(15)\n"
"v_mfma_f32_16x16x32_bf16 v[176:179], v[24:27], v[192:195], 0\n"
"v_mfma_f32_16x16x32_bf16 v[180:183], v[28:31], v[192:195], 0\n"
"v_mfma_f32_16x16x32_bf16 v[184:187], v[32:35], v[192:195], 0\n"
"v_mfma_f32_16x16x32_bf16 v[188:191], v[36:39], v[192:195], 0\n"
"s_waitcnt vmcnt(14)\n"
"s_nop 0\n"
"v_mfma_f32_16x16x32_bf16 v[176:179], v[40:43], v[196:199], v[176:179]\n"
"ds_read_b64_tr_b16 v[24:25], v18 offset:32768\n"
"ds_read_b64_tr_b16 v[26:27], v18 offset:34816\n"
"v_mfma_f32_16x16x32_bf16 v[180:183], v[44:47], v[196:199], v[180:183]\n"
"ds_read_b64_tr_b16 v[28:29], v19 offset:32768\n"
"ds_read_b64_tr_b16 v[30:31], v19 offset:34816\n"
"v_mfma_f32_16x16x32_bf16 v[184:187], v[48:51], v[196:199], v[184:187]\n"
"ds_read_b64_tr_b16 v[32:33], v20 offset:32768\n"
"ds_read_b64_tr_b16 v[34:35], v20 offset:34816\n"
"v_mfma_f32_16x16x32_bf16 v[188:191], v[52:55], v[196:199], v[188:191]\n"
"ds_read_b64_tr_b16 v[36:37], v21 offset:32768\n"
"ds_read_b64_tr_b16 v[38:39], v21 offset:34816\n"
"s_waitcnt vmcnt(13)\n"
"s_nop 0\n"
"v_mfma_f32_16x16x32_bf16 v[176:179], v[56:59], v[200:203], v[176:179]\n"
"ds_read_b64_tr_b16 v[40:41], v18 offset:36864\n"
"ds_read_b64_tr_b16 v[42:43], v18 offset:38912\n"
"v_mfma_f32_16x16x32_bf16 v[180:183], v[60:63], v[200:203], v[180:183]\n"
"ds_read_b64_tr_b16 v[44:45], v19 offset:36864\n"
"ds_read_b64_tr_b16 v[46:47], v19 offset:38912\n"
"v_mfma_f32_16x16x32_bf16 v[184:187], v[64:67], v[200:203], v[184:187]\n"
"ds_read_b64_tr_b16 v[48:49], v20 offset:36864\n"
"ds_read_b64_tr_b16 v[50:51], v20 offset:38912\n"
"v_mfma_f32_16x16x32_bf16 v[188:191], v[68:71], v[200:203], v[188:191]\n"
"ds_read_b64_tr_b16 v[52:53], v21 offset:36864\n"
"ds_read_b64_tr_b16 v[54:55], v21 offset:38912\n"
"s_waitcnt vmcnt(12)\n"
"s_nop 0\n"
"v_mfma_f32_16x16x32_bf16 v[176:179], v[72:75], v[204:207], v[176:179]\n"
"ds_read_b64_tr_b16 v[56:57], v18 offset:40960\n"
"ds_read_b64_tr_b16 v[58:59], v18 offset:43008\n"
"v_mfma_f32_16x16x32_bf16 v[180:183], v[76:79], v[204:207], v[180:183]\n"
"ds_read_b64_tr_b16 v[60:61], v19 offset:40960\n"
"ds_read_b64_tr_b16 v[62:63], v19 offset:43008\n"
"v_mfma_f32_16x16x32_bf16 v[184:187], v[80:83], v[204:207], v[184:187]\n"
"ds_read_b64_tr_b16 v[64:65], v20 offset:40960\n"
"ds_read_b64_tr_b16 v[66:67], v20 offset:43008\n"
"v_mfma_f32_16x16x32_bf16 v[188:191], v[84:87], v[204:207], v[188:191]\n"
"ds_read_b64_tr_b16 v[68:69], v21 offset:40960\n"
"ds_read_b64_tr_b16 v[70:71], v21 offset:43008\n"
"s_waitcnt vmcnt(11)\n"
"s_nop 0\n"
"v_mfma_f32_16x16x32_bf16 v[176:179], v[88:91], v[208:211], v[176:179]\n"
"ds_read_b64_tr_b16 v[72:73], v18 offset:45056\n"
"ds_read_b64_tr_b16 v[74:75], v18 offset:47104\n"
"v_mfma_f32_16x16x32_bf16 v[180:183], v[92:95], v[208:211], v[180:183]\n"
"ds_read_b64_tr_b16 v[76:77], v19 offset:45056\n"
"ds_read_b64_tr_b16 v[78:79], v19 offset:47104\n"
"v_mfma_f32_16x16x32_bf16 v[184:187], v[96:99], v[208:211], v[184:187]\n"
"ds_read_b64_tr_b16 v[80:81], v20 offset:45056\n"
"ds_read_b64_tr_b16 v[82:83], v20 offset:47104\n"
"v_mfma_f32_16x16x32_bf16 v[188:191], v[100:103], v[208:211], v[188:191]\n"
"ds_read_b64_tr_b16 v[84:85], v21 offset:45056\n"
"ds_read_b64_tr_b16 v[86:87], v21 offset:47104\n"
"s_waitcnt vmcnt(10)\n"
"s_nop 0\n"
"v_mfma_f32_16x16x32_bf16 v[176:179], v[104:107], v[212:215], v[176:179]\n"
"ds_read_b64_tr_b16 v[88:89], v18 offset:49152\n"
"ds_read_b64_tr_b16 v[90:91], v18 offset:51200\n"
"v_mfma_f32_16x16x32_bf16 v[180:183], v[108:111], v[212:215], v[180:183]\n"
"ds_read_b64_tr_b16 v[92:93], v19 offset:49152\n"
"ds_read_b64_tr_b16 v[94:95], v19 offset:51200\n"
"v_mfma_f32_16x16x32_bf16 v[184:187], v[112:115], v[212:215], v[184:187]\n"
"ds_read_b64_tr_b16 v[96:97], v20 offset:49152\n"
"ds_read_b64_tr_b16 v[98:99], v20 offset:51200\n"
"v_mfma_f32_16x16x32_bf16 v[188:191], v[116:119], v[212:215], v[188:191]\n"
"ds_read_b64_tr_b16 v[100:101], v21 offset:49152\n"
"ds_read_b64_tr_b16 v[102:103], v21 offset:51200\n"
"s_waitcnt vmcnt(9)\n"
"s_nop 0\n"
"v_mfma_f32_16x16x32_bf16 v[176:179], v[120:123], v[216:219], v[176:179]\n"
"ds_read_b64_tr_b16 v[104:105], v18 offset:53248\n"
"ds_read_b64_tr_b16 v[106:107], v18 offset:55296\n"
"v_mfma_f32_16x16x32_bf16 v[180:183], v[124:127], v[216:219], v[180:183]\n"
"ds_read_b64_tr_b16 v[108:109], v19 offset:53248\n"
"ds_read_b64_tr_b16 v[110:111], v19 offset:55296\n"
"v_mfma_f32_16x16x32_bf16 v[184:187], v[128:131], v[216:219], v[184:187]\n"
"ds_read_b64_tr_b16 v[112:113], v20 offset:53248\n"
"ds_read_b64_tr_b16 v[114:115], v20 offset:55296\n"
"v_mfma_f32_16x16x32_bf16 v[188:191], v[132:135], v[216:219], v[188:191]\n"
"ds_read_b64_tr_b16 v[116:117], v21 offset:53248\n"
"ds_read_b64_tr_b16 v[118:119], v21 offset:55296\n"
"s_waitcnt vmcnt(8)\n"
"s_nop 0\n"
"v_mfma_f32_16x16x32_bf16 v[176:179], v[136:139], v[220:223], v[176:179]\n"
"ds_read_b64_tr_b16 v[120:121], v18 offset:57344\n"
"ds_read_b64_tr_b16 v[122:123], v18 offset:59392\n"
"v_mfma_f32_16x16x32_bf16 v[180:183], v[140:143], v[220:223], v[180:183]\n"
"ds_read_b64_tr_b16 v[124:125], v19 offset:57344\n"
"ds_read_b64_tr_b16 v[126:127], v19 offset:59392\n"
"v_mfma_f32_16x16x32_bf16 v[184:187], v[144:147], v[220:223], v[184:187]\n"
"ds_read_b64_tr_b16 v[128:129], v20 offset:57344\n"
"ds_read_b64_tr_b16 v[130:131], v20 offset:59392\n"
"v_mfma_f32_16x16x32_bf16 v[188:191], v[148:151], v[220:223], v[188:191]\n"
"ds_read_b64_tr_b16 v[132:133], v21 offset:57344\n"
"ds_read_b64_tr_b16 v[134:135], v21 offset:59392\n"
"s_waitcnt vmcnt(7)\n"
"s_waitcnt lgkmcnt(8)\n"
"v_mfma_f32_16x16x32_bf16 v[176:179], v[24:27], v[224:227], v[176:179]\n"
"ds_read_b64_tr_b16 v[136:137], v18 offset:61440\n"
"ds_read_b64_tr_b16 v[138:139], v18 offset:63488\n"
"v_mfma_f32_16x16x32_bf16 v[180:183], v[28:31], v[224:227], v[180:183]\n"
"ds_read_b64_tr_b16 v[140:141], v19 offset:61440\n"
"ds_read_b64_tr_b16 v[142:143], v19 offset:63488\n"
"v_mfma_f32_16x16x32_bf16 v[184:187], v[32:35], v[224:227], v[184:187]\n"
"ds_read_b64_tr_b16 v[144:145], v20 offset:61440\n"
"ds_read_b64_tr_b16 v[146:147], v20 offset:63488\n"
"v_mfma_f32_16x16x32_bf16 v[188:191], v[36:39], v[224:227], v[188:191]\n"
"ds_read_b64_tr_b16 v[148:149], v21 offset:61440\n"
"ds_read_b64_tr_b16 v[150:151], v21 offset:63488\n"
"s_waitcnt vmcnt(6)\n"
"s_nop 0\n"
"v_mfma_f32_16x16x32_bf16 v[176:179], v[40:43], v[228:231], v[176:179]\n"
"v_mfma_f32_16x16x32_bf16 v[180:183], v[44:47], v[228:231], v[180:183]\n"
"v_mfma_f32_16x16x32_bf16 v[184:187], v[48:51], v[228:231], v[184:187]\n"
"v_mfma_f32_16x16x32_bf16 v[188:191], v[52:55], v[228:231], v[188:191]\n"
"s_waitcnt vmcnt(5)\n"
"s_nop 0\n"
"v_mfma_f32_16x16x32_bf16 v[176:179], v[56:59], v[232:235], v[176:179]\n"
"v_mfma_f32_16x16x32_bf16 v[180:183], v[60:63], v[232:235], v[180:183]\n"
"v_mfma_f32_16x16x32_bf16 v[184:187], v[64:67], v[232:235], v[184:187]\n"
"v_mfma_f32_16x16x32_bf16 v[188:191], v[68:71], v[232:235], v[188:191]\n"
"s_waitcnt vmcnt(4)\n"
"s_nop 0\n"
"v_mfma_f32_16x16x32_bf16 v[176:179], v[72:75], v[236:239], v[176:179]\n"
"v_mfma_f32_16x16x32_bf16 v[180:183], v[76:79], v[236:239], v[180:183]\n"
"v_mfma_f32_16x16x32_bf16 v[184:187], v[80:83], v[236:239], v[184:187]\n"
"v_mfma_f32_16x16x32_bf16 v[188:191], v[84:87], v[236:239], v[188:191]\n"
"s_waitcnt vmcnt(3)\n"
"s_waitcnt lgkmcnt(0)\n"
"v_mfma_f32_16x16x32_bf16 v[176:179], v[88:91], v[240:243], v[176:179]\n"
"v_mfma_f32_16x16x32_bf16 v[180:183], v[92:95], v[240:243], v[180:183]\n"
"v_mfma_f32_16x16x32_bf16 v[184:187], v[96:99], v[240:243], v[184:187]\n"
"v_mfma_f32_16x16x32_bf16 v[188:191], v[100:103], v[240:243], v[188:191]\n"
"s_waitcnt vmcnt(2)\n"
"s_nop 0\n"
"v_mfma_f32_16x16x32_bf16 v[176:179], v[104:107], v[244:247], v[176:179]\n"
"v_mfma_f32_16x16x32_bf16 v[180:183], v[108:111], v[244:247], v[180:183]\n"
"v_mfma_f32_16x16x32_bf16 v[184:187], v[112:115], v[244:247], v[184:187]\n"
"v_mfma_f32_16x16x32_bf16 v[188:191], v[116:119], v[244:247], v[188:191]\n"
"s_waitcnt vmcnt(1)\n"
"s_nop 0\n"
"v_mfma_f32_16x16x32_bf16 v[176:179], v[120:123], v[248:251], v[176:179]\n"
"ds_read_b32 v14, v13\n"
"ds_read_b128 v[24:27], v12 offset:0\n"
"v_mfma_f32_16x16x32_bf16 v[180:183], v[124:127], v[248:251], v[180:183]\n"
"ds_read_b128 v[28:31], v12 offset:1024\n"
"ds_read_b128 v[32:35], v12 offset:2048\n"
"v_mfma_f32_16x16x32_bf16 v[184:187], v[128:131], v[248:251], v[184:187]\n"
"ds_read_b128 v[36:39], v12 offset:3072\n"
"v_mfma_f32_16x16x32_bf16 v[188:191], v[132:135], v[248:251], v[188:191]\n"
"s_waitcnt vmcnt(0)\n"
"s_nop 0\n"
"v_mfma_f32_16x16x32_bf16 v[176:179], v[136:139], v[252:255], v[176:179]\n"
"v_mfma_f32_16x16x32_bf16 v[180:183], v[140:143], v[252:255], v[180:183]\n"
"v_mfma_f32_16x16x32_bf16 v[184:187], v[144:147], v[252:255], v[184:187]\n"
"v_mfma_f32_16x16x32_bf16 v[188:191], v[148:151], v[252:255], v[188:191]\n"
".Lfb_C_GC:\n"
"s_waitcnt lgkmcnt(0)\n"
"v_cmp_gt_u32_e64 vcc, 5, v14\n"
"s_nop 0\n"
"s_cbranch_vccz .Lfb_C_GO\n"
"s_sleep 0\n"
"ds_read_b32 v14, v13\n"
"ds_read_b128 v[24:27], v12 offset:0\n"
"ds_read_b128 v[28:31], v12 offset:1024\n"
"ds_read_b128 v[32:35], v12 offset:2048\n"
"ds_read_b128 v[36:39], v12 offset:3072\n"
"s_sub_u32 s31, s31, 1\n"
"s_cbranch_scc0 .Lfb_C_GC\n"
"s_mov_b32 s31, 0\n"
"s_waitcnt lgkmcnt(0)\n"
".Lfb_C_GO:\n"
"s_nop 0\n"
"s_nop 2\n"
"v_pk_fma_f32 v[152:153], v[176:177], v[24:25], v[152:153]\n"
"v_pk_fma_f32 v[154:155], v[178:179], v[26:27], v[154:155]\n"
"v_cvt_pk_bf16_f32 v40, v152, v153\n"
"v_cvt_pk_bf16_f32 v41, v154, v155\n"
"ds_write_b64 v172, v[40:41]\n"
"v_pk_fma_f32 v[156:157], v[180:181], v[28:29], v[156:157]\n"
"v_pk_fma_f32 v[158:159], v[182:183], v[30:31], v[158:159]\n"
"v_cvt_pk_bf16_f32 v42, v156, v157\n"
"v_cvt_pk_bf16_f32 v43, v158, v159\n"
"ds_write_b64 v173, v[42:43]\n"
"v_pk_fma_f32 v[160:161], v[184:185], v[32:33], v[160:161]\n"
"v_pk_fma_f32 v[162:163], v[186:187], v[34:35], v[162:163]\n"
"v_cvt_pk_bf16_f32 v44, v160, v161\n"
"v_cvt_pk_bf16_f32 v45, v162, v163\n"
"ds_write_b64 v174, v[44:45]\n"
"v_pk_fma_f32 v[164:165], v[188:189], v[36:37], v[164:165]\n"
"v_pk_fma_f32 v[166:167], v[190:191], v[38:39], v[166:167]\n"
"v_cvt_pk_bf16_f32 v46, v164, v165\n"
"v_cvt_pk_bf16_f32 v47, v166, v167\n"
"ds_write_b64 v175, v[46:47]\n"
"ds_read_b128 v[48:51], v5 offset:0\n"
"ds_read_b128 v[52:55], v5 offset:1024\n"
"s_waitcnt lgkmcnt(1)\n"
"global_store_dwordx4 v3, v[48:51], s[54:55] sc1\n"
"s_waitcnt lgkmcnt(0)\n"
"global_store_dwordx4 v4, v[52:55], s[54:55] sc1\n"
"s_endpgm\n"
".Lfb_C_IDLE:\n"
"s_barrier\n"
"ds_read_b64 v[10:11], v9 offset:32\n"
"s_waitcnt vmcnt(0) lgkmcnt(0)\n"
"v_readfirstlane_b32 s32, v10\n"
"v_readfirstlane_b32 s33, v11\n"
"v_lshl_add_u32 v12, s20, 2, v9\n"
"v_mov_b32 v13, 3\n"
"ds_write_b32 v12, v13 offset:16\n"
"v_lshl_add_u32 v20, s20, 6, v0\n"
"v_mov_b32_e64 v21, 0\n"
"s_cmp_eq_u32 s24, 0\n"
"s_cbranch_scc1 .Lfb_C_W0\n"
"s_and_b32 s34, s26, 1\n"
"s_lshl_b32 s34, s34, 17\n"
"s_add_u32 s34, s34, 10485760\n"
"v_lshrrev_b32 v22, 5, v20\n"
"v_and_b32 v23, 31, v20\n"
"v_lshlrev_b32_e64 v22, 13, v22\n"
"v_lshl_add_u32 v22, v23, 6, v22\n"
"v_add_u32_e64 v22, s34, v22\n"
"global_atomic_or v22, v21, s[18:19] sc1\n"
"v_add_u32 v22, 0x10000, v22\n"
"global_atomic_or v22, v21, s[18:19] sc1\n"
"v_add_u32 v22, 0x10000, v22\n"
"s_branch .Lfb_C_W1\n"
".Lfb_C_W0:\n"
"v_lshlrev_b32 v22, 6, v20\n"
"v_add_u32 v22, 0x980000, v22\n"
"global_atomic_or v22, v21, s[18:19] sc1\n"
"v_add_u32 v22, 0x4000, v22\n"
"global_atomic_or v22, v21, s[18:19] sc1\n"
"v_add_u32 v22, 0x4000, v22\n"
"global_atomic_or v22, v21, s[18:19] sc1\n"
"v_add_u32 v22, 0x4000, v22\n"
"global_atomic_or v22, v21, s[18:19] sc1\n"
"v_add_u32 v22, 0x4000, v22\n"
"v_lshlrev_b32_e64 v22, 6, v20\n"
"v_add_u32 v22, 0x900000, v22\n"
"global_atomic_or v22, v21, s[18:19] sc1\n"
"v_add_u32 v22, 0x4000, v22\n"
"global_atomic_or v22, v21, s[18:19] sc1\n"
"v_add_u32 v22, 0x4000, v22\n"
"global_atomic_or v22, v21, s[18:19] sc1\n"
"v_add_u32 v22, 0x4000, v22\n"
"global_atomic_or v22, v21, s[18:19] sc1\n"
"v_add_u32 v22, 0x4000, v22\n"
"global_atomic_or v22, v21, s[18:19] sc1\n"
"v_add_u32 v22, 0x4000, v22\n"
"global_atomic_or v22, v21, s[18:19] sc1\n"
"v_add_u32 v22, 0x4000, v22\n"
"global_atomic_or v22, v21, s[18:19] sc1\n"
"v_add_u32 v22, 0x4000, v22\n"
"global_atomic_or v22, v21, s[18:19] sc1\n"
"v_add_u32 v22, 0x4000, v22\n"
".Lfb_C_W1:\n"
"s_or_b32 s34, s24, s20\n"
"s_nop 0\n"
"s_nop 3\n"
"s_cbranch_scc1 .Lfb_C_P2\n"
"s_add_u32 s52, s18, 65536\n"
"s_addc_u32 s53, s19, 0\n"
"s_mov_b32 s48, 4\n"
"v_lshl_add_u32 v24, v0, 8, 12\n"
".Lfb_C_EP:\n"
"global_load_dword v25, v24, s[52:53] sc1\n"
"s_waitcnt vmcnt(0)\n"
"v_cmp_ne_u32 vcc, s32, v25\n"
"s_nop 0\n"
"s_cbranch_vccz .Lfb_C_EN\n"
"s_sleep 16\n"
"s_sub_u32 s31, s31, 1\n"
"s_cbranch_scc0 .Lfb_C_EP\n"
"s_mov_b32 s31, 0\n"
".Lfb_C_EN:\n"
"v_add_u32 v24, 0x4000, v24\n"
"s_sub_u32 s48, s48, 1\n"
"s_cmp_lg_u32 s48, 0\n"
"s_cbranch_scc1 .Lfb_C_EP\n"
"v_mov_b32 v26, s32\n"
"v_mov_b32 v27, s33\n"
"v_mov_b32 v28, 0\n"
"s_mov_b64 exec, 1\n"
"s_waitcnt vmcnt(0)\n"
"global_store_dwordx2 v28, v[26:27], s[18:19] sc1\n"
"s_mov_b64 exec, -1\n"
"s_branch .Lfb_C_P2\n"
".Lfb_C_LS:\n"
"s_barrier\n"
".Lfb_C_LSL:\n"
"s_nop 0\n"
"ds_read_b128 v[16:19], v1 offset:0\n"
"ds_read_b128 v[20:23], v1 offset:2048\n"
"ds_read_b128 v[24:27], v1 offset:4096\n"
"ds_read_b64_tr_b16 v[48:49], v3 offset:64\n"
"ds_read_b64_tr_b16 v[50:51], v3 offset:832\n"
"ds_read_b128 v[28:31], v1 offset:6144\n"
"ds_read_b128 v[32:35], v1 offset:8192\n"
"ds_read_b128 v[36:39], v1 offset:10240\n"
"ds_read_b64_tr_b16 v[60:61], v3 offset:12352\n"
"ds_read_b64_tr_b16 v[62:63], v3 offset:13120\n"
"ds_read_b128 v[64:67], v2 offset:0\n"
"ds_read_b128 v[68:71], v2 offset:2048\n"
"ds_read_b128 v[72:75], v2 offset:4096\n"
"ds_read_b64_tr_b16 v[96:97], v3 offset:6208\n"
"ds_read_b64_tr_b16 v[98:99], v3 offset:6976\n"
"ds_read_b128 v[76:79], v2 offset:6144\n"
"ds_read_b128 v[80:83], v2 offset:8192\n"
"ds_read_b128 v[84:87], v2 offset:10240\n"
"ds_read_b64_tr_b16 v[108:109], v3 offset:18496\n"
"ds_read_b64_tr_b16 v[110:111], v3 offset:19264\n"
"s_add_u32 s50, s50, 1\n"
"s_cmp_eq_u32 s50, 3\n"
"s_cselect_b32 s50, 0, s50\n"
"s_mov_b32 s49, 49152\n"
"s_cselect_b32 s49, 4294868992, s49\n"
"v_add_u32 v1, s49, v1\n"
"v_add_u32 v2, s49, v2\n"
"v_add_u32 v3, s49, v3\n"
"v_add_u32 v4, s49, v4\n"
"s_sub_u32 s48, s48, 1\n"
"s_cmp_lg_u32 s48, 0\n"
"s_waitcnt lgkmcnt(0)\n"
"v_mfma_f32_16x16x32_bf16 v[160:163], v[48:51], v[16:19], v[160:163]\n"
"v_mfma_f32_16x16x32_bf16 v[232:235], v[60:63], v[16:19], v[232:235]\n"
"v_mfma_f32_16x16x32_bf16 v[164:167], v[48:51], v[20:23], v[164:167]\n"
"v_mfma_f32_16x16x32_bf16 v[236:239], v[60:63], v[20:23], v[236:239]\n"
"v_mfma_f32_16x16x32_bf16 v[168:171], v[48:51], v[24:27], v[168:171]\n"
"v_mfma_f32_16x16x32_bf16 v[240:243], v[60:63], v[24:27], v[240:243]\n"
"v_mfma_f32_16x16x32_bf16 v[172:175], v[48:51], v[28:31], v[172:175]\n"
"v_mfma_f32_16x16x32_bf16 v[244:247], v[60:63], v[28:31], v[244:247]\n"
"v_mfma_f32_16x16x32_bf16 v[176:179], v[48:51], v[32:35], v[176:179]\n"
"v_mfma_f32_16x16x32_bf16 v[248:251], v[60:63], v[32:35], v[248:251]\n"
"v_mfma_f32_16x16x32_bf16 v[180:183], v[48:51], v[36:39], v[180:183]\n"
"v_mfma_f32_16x16x32_bf16 v[252:255], v[60:63], v[36:39], v[252:255]\n"
"v_mfma_f32_16x16x32_bf16 v[160:163], v[96:99], v[64:67], v[160:163]\n"
"v_mfma_f32_16x16x32_bf16 v[232:235], v[108:111], v[64:67], v[232:235]\n"
"v_mfma_f32_16x16x32_bf16 v[164:167], v[96:99], v[68:71], v[164:167]\n"
"v_mfma_f32_16x16x32_bf16 v[236:239], v[108:111], v[68:71], v[236:239]\n"
"v_mfma_f32_16x16x32_bf16 v[168:171], v[96:99], v[72:75], v[168:171]\n"
"v_mfma_f32_16x16x32_bf16 v[240:243], v[108:111], v[72:75], v[240:243]\n"
"v_mfma_f32_16x16x32_bf16 v[172:175], v[96:99], v[76:79], v[172:175]\n"
"v_mfma_f32_16x16x32_bf16 v[244:247], v[108:111], v[76:79], v[244:247]\n"
"v_mfma_f32_16x16x32_bf16 v[176:179], v[96:99], v[80:83], v[176:179]\n"
"v_mfma_f32_16x16x32_bf16 v[248:251], v[108:111], v[80:83], v[248:251]\n"
"v_mfma_f32_16x16x32_bf16 v[180:183], v[96:99], v[84:87], v[180:183]\n"
"v_mfma_f32_16x16x32_bf16 v[252:255], v[108:111], v[84:87], v[252:255]\n"
"s_barrier\n"
"s_cbranch_scc1 .Lfb_C_LSL\n"
"ds_read_b128 v[16:19], v1 offset:0\n"
"ds_read_b64_tr_b16 v[40:41], v3 offset:0\n"
"ds_read_b64_tr_b16 v[42:43], v3 offset:768\n"
"ds_read_b128 v[20:23], v1 offset:2048\n"
"ds_read_b64_tr_b16 v[44:45], v4 offset:32\n"
"ds_read_b64_tr_b16 v[46:47], v4 offset:800\n"
"ds_read_b128 v[24:27], v1 offset:4096\n"
"ds_read_b64_tr_b16 v[48:49], v3 offset:64\n"
"ds_read_b64_tr_b16 v[50:51], v3 offset:832\n"
"ds_read_b128 v[28:31], v1 offset:6144\n"
"ds_read_b64_tr_b16 v[52:53], v3 offset:12288\n"
"ds_read_b64_tr_b16 v[54:55], v3 offset:13056\n"
"ds_read_b128 v[32:35], v1 offset:8192\n"
"ds_read_b64_tr_b16 v[56:57], v4 offset:12320\n"
"ds_read_b64_tr_b16 v[58:59], v4 offset:13088\n"
"ds_read_b128 v[36:39], v1 offset:10240\n"
"ds_read_b64_tr_b16 v[60:61], v3 offset:12352\n"
"ds_read_b64_tr_b16 v[62:63], v3 offset:13120\n"
"s_branch .Lfb_C_LEG\n"
".Lfb_C_LPG:\n"
"s_barrier\n"
".Lfb_C_LPGL:\n"
"ds_read_b128 v[16:19], v1 offset:0\n"
"ds_read_b64_tr_b16 v[40:41], v3 offset:0\n"
"ds_read_b64_tr_b16 v[42:43], v3 offset:768\n"
"ds_read_b128 v[20:23], v1 offset:2048\n"
"ds_read_b64_tr_b16 v[44:45], v4 offset:32\n"
"ds_read_b64_tr_b16 v[46:47], v4 offset:800\n"
"ds_read_b64_tr_b16 v[48:49], v3 offset:64\n"
"ds_read_b64_tr_b16 v[50:51], v3 offset:832\n"
"ds_read_b64_tr_b16 v[52:53], v3 offset:12288\n"
"ds_read_b64_tr_b16 v[54:55], v3 offset:13056\n"
"ds_read_b64_tr_b16 v[56:57], v4 offset:12320\n"
"ds_read_b64_tr_b16 v[58:59], v4 offset:13088\n"
"ds_read_b64_tr_b16 v[60:61], v3 offset:12352\n"
"ds_read_b64_tr_b16 v[62:63], v3 offset:13120\n"
"ds_read_b128 v[64:67], v2 offset:0\n"
"ds_read_b64_tr_b16 v[88:89], v3 offset:6144\n"
"ds_read_b64_tr_b16 v[90:91], v3 offset:6912\n"
"ds_read_b128 v[68:71], v2 offset:2048\n"
"ds_read_b64_tr_b16 v[92:93], v4 offset:6176\n"
"ds_read_b64_tr_b16 v[94:95], v4 offset:6944\n"
"ds_read_b64_tr_b16 v[96:97], v3 offset:6208\n"
"ds_read_b64_tr_b16 v[98:99], v3 offset:6976\n"
"ds_read_b64_tr_b16 v[100:101], v3 offset:18432\n"
"ds_read_b64_tr_b16 v[102:103], v3 offset:19200\n"
"ds_read_b64_tr_b16 v[104:105], v4 offset:18464\n"
"ds_read_b64_tr_b16 v[106:107], v4 offset:19232\n"
"ds_read_b64_tr_b16 v[108:109], v3 offset:18496\n"
"ds_read_b64_tr_b16 v[110:111], v3 offset:19264\n"
"s_add_u32 s50, s50, 1\n"
"s_cmp_eq_u32 s50, 3\n"
"s_cselect_b32 s50, 0, s50\n"
"s_mov_b32 s49, 49152\n"
"s_cselect_b32 s49, 4294868992, s49\n"
"v_add_u32 v1, s49, v1\n"
"v_add_u32 v2, s49, v2\n"
"v_add_u32 v3, s49, v3\n"
"v_add_u32 v4, s49, v4\n"
"s_sub_u32 s48, s48, 1\n"
"s_cmp_lg_u32 s48, 0\n"
"s_waitcnt lgkmcnt(0)\n"
"v_mfma_f32_16x16x32_bf16 v[112:115], v[40:43], v[16:19], v[112:115]\n"
"v_mfma_f32_16x16x32_bf16 v[136:139], v[44:47], v[16:19], v[136:139]\n"
"v_mfma_f32_16x16x32_bf16 v[160:163], v[48:51], v[16:19], v[160:163]\n"
"v_mfma_f32_16x16x32_bf16 v[184:187], v[52:55], v[16:19], v[184:187]\n"
"v_mfma_f32_16x16x32_bf16 v[208:211], v[56:59], v[16:19], v[208:211]\n"
"v_mfma_f32_16x16x32_bf16 v[232:235], v[60:63], v[16:19], v[232:235]\n"
"v_mfma_f32_16x16x32_bf16 v[116:119], v[40:43], v[20:23], v[116:119]\n"
"v_mfma_f32_16x16x32_bf16 v[140:143], v[44:47], v[20:23], v[140:143]\n"
"v_mfma_f32_16x16x32_bf16 v[164:167], v[48:51], v[20:23], v[164:167]\n"
"v_mfma_f32_16x16x32_bf16 v[188:191], v[52:55], v[20:23], v[188:191]\n"
"v_mfma_f32_16x16x32_bf16 v[212:215], v[56:59], v[20:23], v[212:215]\n"
"v_mfma_f32_16x16x32_bf16 v[236:239], v[60:63], v[20:23], v[236:239]\n"
"v_mfma_f32_16x16x32_bf16 v[112:115], v[88:91], v[64:67], v[112:115]\n"
"v_mfma_f32_16x16x32_bf16 v[136:139], v[92:95], v[64:67], v[136:139]\n"
"v_mfma_f32_16x16x32_bf16 v[160:163], v[96:99], v[64:67], v[160:163]\n"
"v_mfma_f32_16x16x32_bf16 v[184:187], v[100:103], v[64:67], v[184:187]\n"
"v_mfma_f32_16x16x32_bf16 v[208:211], v[104:107], v[64:67], v[208:211]\n"
"v_mfma_f32_16x16x32_bf16 v[232:235], v[108:111], v[64:67], v[232:235]\n"
"v_mfma_f32_16x16x32_bf16 v[116:119], v[88:91], v[68:71], v[116:119]\n"
"v_mfma_f32_16x16x32_bf16 v[140:143], v[92:95], v[68:71], v[140:143]\n"
"v_mfma_f32_16x16x32_bf16 v[164:167], v[96:99], v[68:71], v[164:167]\n"
"v_mfma_f32_16x16x32_bf16 v[188:191], v[100:103], v[68:71], v[188:191]\n"
"v_mfma_f32_16x16x32_bf16 v[212:215], v[104:107], v[68:71], v[212:215]\n"
"v_mfma_f32_16x16x32_bf16 v[236:239], v[108:111], v[68:71], v[236:239]\n"
"s_barrier\n"
"s_cbranch_scc1 .Lfb_C_LPGL\n"
"ds_read_b128 v[16:19], v1 offset:0\n"
"ds_read_b64_tr_b16 v[40:41], v3 offset:0\n"
"ds_read_b64_tr_b16 v[42:43], v3 offset:768\n"
"ds_read_b128 v[20:23], v1 offset:2048\n"
"ds_read_b64_tr_b16 v[44:45], v4 offset:32\n"
"ds_read_b64_tr_b16 v[46:47], v4 offset:800\n"
"ds_read_b128 v[24:27], v1 offset:4096\n"
"ds_read_b64_tr_b16 v[48:49], v3 offset:64\n"
"ds_read_b64_tr_b16 v[50:51], v3 offset:832\n"
"ds_read_b128 v[28:31], v1 offset:6144\n"
"ds_read_b64_tr_b16 v[52:53], v3 offset:12288\n"
"ds_read_b64_tr_b16 v[54:55], v3 offset:13056\n"
"ds_read_b128 v[32:35], v1 offset:8192\n"
"ds_read_b64_tr_b16 v[56:57], v4 offset:12320\n"
"ds_read_b64_tr_b16 v[58:59], v4 offset:13088\n"
"ds_read_b128 v[36:39], v1 offset:10240\n"
"ds_read_b64_tr_b16 v[60:61], v3 offset:12352\n"
"ds_read_b64_tr_b16 v[62:63], v3 offset:13120\n"
"s_branch .Lfb_C_LEG\n"
".Lfb_C_LPQ:\n"
"s_barrier\n"
".Lfb_C_LPQL:\n"
"ds_read_b64_tr_b16 v[80:81], v3 offset:0\n"
"ds_read_b64_tr_b16 v[82:83], v3 offset:512\n"
"ds_read_b64_tr_b16 v[84:85], v3 offset:4096\n"
"ds_read_b64_tr_b16 v[86:87], v3 offset:4608\n"
"ds_read_b64_tr_b16 v[88:89], v4 offset:8192\n"
"ds_read_b64_tr_b16 v[90:91], v4 offset:8704\n"
"ds_read_b128 v[16:19], v1 offset:0\n"
"ds_read_b128 v[20:23], v1 offset:2048\n"
"ds_read_b128 v[24:27], v1 offset:4096\n"
"ds_read_b128 v[28:31], v1 offset:6144\n"
"ds_read_b128 v[32:35], v1 offset:8192\n"
"ds_read_b128 v[36:39], v1 offset:10240\n"
"ds_read_b128 v[40:43], v1 offset:12288\n"
"ds_read_b128 v[44:47], v1 offset:14336\n"
"ds_read_b64_tr_b16 v[92:93], v3 offset:12288\n"
"ds_read_b64_tr_b16 v[94:95], v3 offset:12800\n"
"ds_read_b64_tr_b16 v[96:97], v3 offset:16384\n"
"ds_read_b64_tr_b16 v[98:99], v3 offset:16896\n"
"ds_read_b64_tr_b16 v[100:101], v4 offset:20480\n"
"ds_read_b64_tr_b16 v[102:103], v4 offset:20992\n"
"ds_read_b128 v[48:51], v2 offset:0\n"
"ds_read_b128 v[52:55], v2 offset:2048\n"
"ds_read_b128 v[56:59], v2 offset:4096\n"
"ds_read_b128 v[60:63], v2 offset:6144\n"
"ds_read_b128 v[64:67], v2 offset:8192\n"
"ds_read_b128 v[68:71], v2 offset:10240\n"
"ds_read_b128 v[72:75], v2 offset:12288\n"
"ds_read_b128 v[76:79], v2 offset:14336\n"
"s_add_u32 s50, s50, 1\n"
"s_cmp_eq_u32 s50, 3\n"
"s_cselect_b32 s50, 0, s50\n"
"s_mov_b32 s49, 49152\n"
"s_cselect_b32 s49, 4294868992, s49\n"
"v_add_u32 v1, s49, v1\n"
"v_add_u32 v2, s49, v2\n"
"v_add_u32 v3, s49, v3\n"
"v_add_u32 v4, s49, v4\n"
"s_sub_u32 s48, s48, 1\n"
"s_cmp_lg_u32 s48, 0\n"
"s_waitcnt lgkmcnt(14)\n"
"v_mfma_f32_16x16x32_bf16 v[160:163], v[16:19], v[84:87], v[160:163]\n"
"v_mfma_f32_16x16x32_bf16 v[208:211], v[16:19], v[88:91], v[208:211]\n"
"v_mfma_f32_16x16x32_bf16 v[112:115], v[80:83], v[16:19], v[112:115]\n"
"v_mfma_f32_16x16x32_bf16 v[164:167], v[20:23], v[84:87], v[164:167]\n"
"v_mfma_f32_16x16x32_bf16 v[212:215], v[20:23], v[88:91], v[212:215]\n"
"v_mfma_f32_16x16x32_bf16 v[116:119], v[80:83], v[20:23], v[116:119]\n"
"v_mfma_f32_16x16x32_bf16 v[168:171], v[24:27], v[84:87], v[168:171]\n"
"v_mfma_f32_16x16x32_bf16 v[216:219], v[24:27], v[88:91], v[216:219]\n"
"v_mfma_f32_16x16x32_bf16 v[120:123], v[80:83], v[24:27], v[120:123]\n"
"v_mfma_f32_16x16x32_bf16 v[172:175], v[28:31], v[84:87], v[172:175]\n"
"v_mfma_f32_16x16x32_bf16 v[220:223], v[28:31], v[88:91], v[220:223]\n"
"v_mfma_f32_16x16x32_bf16 v[124:127], v[80:83], v[28:31], v[124:127]\n"
"v_mfma_f32_16x16x32_bf16 v[176:179], v[32:35], v[84:87], v[176:179]\n"
"v_mfma_f32_16x16x32_bf16 v[224:227], v[32:35], v[88:91], v[224:227]\n"
"v_mfma_f32_16x16x32_bf16 v[128:131], v[80:83], v[32:35], v[128:131]\n"
"v_mfma_f32_16x16x32_bf16 v[180:183], v[36:39], v[84:87], v[180:183]\n"
"v_mfma_f32_16x16x32_bf16 v[228:231], v[36:39], v[88:91], v[228:231]\n"
"v_mfma_f32_16x16x32_bf16 v[132:135], v[80:83], v[36:39], v[132:135]\n"
"v_mfma_f32_16x16x32_bf16 v[184:187], v[40:43], v[84:87], v[184:187]\n"
"v_mfma_f32_16x16x32_bf16 v[232:235], v[40:43], v[88:91], v[232:235]\n"
"v_mfma_f32_16x16x32_bf16 v[136:139], v[80:83], v[40:43], v[136:139]\n"
"v_mfma_f32_16x16x32_bf16 v[188:191], v[44:47], v[84:87], v[188:191]\n"
"v_mfma_f32_16x16x32_bf16 v[236:239], v[44:47], v[88:91], v[236:239]\n"
"v_mfma_f32_16x16x32_bf16 v[140:143], v[80:83], v[44:47], v[140:143]\n"
"s_waitcnt lgkmcnt(0)\n"
"s_nop 0\n"
"v_mfma_f32_16x16x32_bf16 v[160:163], v[48:51], v[96:99], v[160:163]\n"
"v_mfma_f32_16x16x32_bf16 v[208:211], v[48:51], v[100:103], v[208:211]\n"
"v_mfma_f32_16x16x32_bf16 v[112:115], v[92:95], v[48:51], v[112:115]\n"
"v_mfma_f32_16x16x32_bf16 v[164:167], v[52:55], v[96:99], v[164:167]\n"
"v_mfma_f32_16x16x32_bf16 v[212:215], v[52:55], v[100:103], v[212:215]\n"
"v_mfma_f32_16x16x32_bf16 v[116:119], v[92:95], v[52:55], v[116:119]\n"
"v_mfma_f32_16x16x32_bf16 v[168:171], v[56:59], v[96:99], v[168:171]\n"
"v_mfma_f32_16x16x32_bf16 v[216:219], v[56:59], v[100:103], v[216:219]\n"
"v_mfma_f32_16x16x32_bf16 v[120:123], v[92:95], v[56:59], v[120:123]\n"
"v_mfma_f32_16x16x32_bf16 v[172:175], v[60:63], v[96:99], v[172:175]\n"
"v_mfma_f32_16x16x32_bf16 v[220:223], v[60:63], v[100:103], v[220:223]\n"
"v_mfma_f32_16x16x32_bf16 v[124:127], v[92:95], v[60:63], v[124:127]\n"
"v_mfma_f32_16x16x32_bf16 v[176:179], v[64:67], v[96:99], v[176:179]\n"
"v_mfma_f32_16x16x32_bf16 v[224:227], v[64:67], v[100:103], v[224:227]\n"
"v_mfma_f32_16x16x32_bf16 v[128:131], v[92:95], v[64:67], v[128:131]\n"
"v_mfma_f32_16x16x32_bf16 v[180:183], v[68:71], v[96:99], v[180:183]\n"
"v_mfma_f32_16x16x32_bf16 v[228:231], v[68:71], v[100:103], v[228:231]\n"
"v_mfma_f32_16x16x32_bf16 v[132:135], v[92:95], v[68:71], v[132:135]\n"
"v_mfma_f32_16x16x32_bf16 v[184:187], v[72:75], v[96:99], v[184:187]\n"
"v_mfma_f32_16x16x32_bf16 v[232:235], v[72:75], v[100:103], v[232:235]\n"
"v_mfma_f32_16x16x32_bf16 v[136:139], v[92:95], v[72:75], v[136:139]\n"
"v_mfma_f32_16x16x32_bf16 v[188:191], v[76:79], v[96:99], v[188:191]\n"
"v_mfma_f32_16x16x32_bf16 v[236:239], v[76:79], v[100:103], v[236:239]\n"
"v_mfma_f32_16x16x32_bf16 v[140:143], v[92:95], v[76:79], v[140:143]\n"
"s_barrier\n"
"s_cbranch_scc1 .Lfb_C_LPQL\n"
"ds_read_b64_tr_b16 v[64:65], v3 offset:0\n"
"ds_read_b64_tr_b16 v[66:67], v3 offset:512\n"
"ds_read_b64_tr_b16 v[68:69], v3 offset:4096\n"
"ds_read_b64_tr_b16 v[70:71], v3 offset:4608\n"
"ds_read_b64_tr_b16 v[72:73], v4 offset:8192\n"
"ds_read_b64_tr_b16 v[74:75], v4 offset:8704\n"
"ds_read_b128 v[16:19], v1 offset:0\n"
"ds_read_b128 v[20:23], v1 offset:2048\n"
"ds_read_b128 v[24:27], v1 offset:4096\n"
"ds_read_b128 v[28:31], v1 offset:6144\n"
"ds_read_b128 v[32:35], v1 offset:8192\n"
"ds_read_b128 v[36:39], v1 offset:10240\n"
"ds_read_b128 v[40:43], v1 offset:12288\n"
"ds_read_b128 v[44:47], v1 offset:14336\n"
"ds_read_b128 v[48:51], v1 offset:16384\n"
"ds_read_b128 v[52:55], v1 offset:18432\n"
"ds_read_b128 v[56:59], v1 offset:20480\n"
"s_branch .Lfb_C_LEQ\n"
".Lfb_EQ_GR:\n"
"s_sub_u32 s31, s31, 1\n"
"s_cbranch_scc0 .Lfb_EQ_GT\n"
"s_mov_b32 s31, 0\n"
"s_branch .Lfb_EQ_GD\n"
".Lfb_EQ_GT:\n"
"s_sleep 1\n"
"v_mov_b32 v52, s70\n"
"v_mov_b32 v53, s71\n"
"v_mov_b32 v54, s72\n"
"v_mov_b32 v55, s73\n"
"s_mov_b32 exec_lo, 0x07ff07ff\n"
"s_mov_b32 exec_hi, 0x07ff07ff\n"
"global_load_dwordx4 v[52:55], v9, s[54:55] sc1\n"
"s_mov_b64 exec, -1\n"
"s_nop 0\n"
"global_load_dwordx4 v[160:163], v5, s[52:53] offset:0 sc1\n"
"global_load_dwordx4 v[164:167], v5, s[52:53] offset:1024 sc1\n"
"global_load_dwordx4 v[168:171], v5, s[52:53] offset:2048 sc1\n"
"global_load_dwordx4 v[172:175], v5, s[52:53] offset:3072 sc1\n"
"global_load_dwordx4 v[176:179], v8, s[52:53] offset:0 sc1\n"
"global_load_dwordx4 v[180:183], v8, s[52:53] offset:1024 sc1\n"
"global_load_dwordx4 v[184:187], v8, s[52:53] offset:2048 sc1\n"
"global_load_dwordx4 v[188:191], v8, s[52:53] offset:3072 sc1\n"
"global_load_dwordx4 v[192:195], v6, s[52:53] offset:0 sc1\n"
"global_load_dwordx4 v[196:199], v6, s[52:53] offset:1024 sc1\n"
"global_load_dwordx4 v[200:203], v6, s[52:53] offset:2048 sc1\n"
"s_branch .Lfb_EQ_GS\n"
".Lfb_KEND:\n"
  :: "{s[0:1]}"(__builtin_amdgcn_kernarg_segment_ptr()), "{s2}"(__builtin_amdgcn_workgroup_id_x()), "{v0}"(__builtin_amdgcn_workitem_id_x())
  : "v1","v2","v3","v4","v5","v6","v7","v8","v9","v10","v11","v12","v13","v14","v15","v16","v17","v18","v19","v20","v21","v22","v23","v24","v25","v26","v27","v28","v29","v30","v31","v32","v33","v34","v35","v36","v37","v38","v39","v40","v41","v42","v43","v44","v45","v46","v47","v48","v49","v50","v51","v52","v53","v54","v55","v56","v57","v58","v59","v60","v61","v62","v63","v64","v65","v66","v67","v68","v69","v70","v71","v72","v73","v74","v75","v76","v77","v78","v79","v80","v81","v82","v83","v84","v85","v86","v87","v88","v89","v90","v91","v92","v93","v94","v95","v96","v97","v98","v99","v100","v101","v102","v103","v104","v105","v106","v107","v108","v109","v110","v111","v112","v113","v114","v115","v116","v117","v118","v119","v120","v121","v122","v123","v124","v125","v126","v127","v128","v129","v130","v131","v132","v133","v134","v135","v136","v137","v138","v139","v140","v141","v142","v143","v144","v145","v146","v147","v148","v149","v150","v151","v152","v153","v154","v155","v156","v157","v158","v159","v160","v161","v162","v163","v164","v165","v166","v167","v168","v169","v170","v171","v172","v173","v174","v175","v176","v177","v178","v179","v180","v181","v182","v183","v184","v185","v186","v187","v188","v189","v190","v191","v192","v193","v194","v195","v196","v197","v198","v199","v200","v201","v202","v203","v204","v205","v206","v207","v208","v209","v210","v211","v212","v213","v214","v215","v216","v217","v218","v219","v220","v221","v222","v223","v224","v225","v226","v227","v228","v229","v230","v231","v232","v233","v234","v235","v236","v237","v238","v239","v240","v241","v242","v243","v244","v245","v246","v247","v248","v249","v250","v251","v252","v253","v254","v255","s3","s4","s5","s6","s7","s8","s9","s10","s11","s12","s13","s14","s15","s16","s17","s18","s19","s20","s21","s22","s23","s24","s25","s26","s27","s28","s29","s30","s31","s32","s33","s34","s35","s36","s37","s38","s39","s40","s41","s42","s43","s44","s45","s46","s47","s48","s49","s50","s51","s52","s53","s54","s55","s56","s57","s58","s59","s60","s61","s62","s63","s64","s65","s66","s67","s68","s69","s70","s71","s72","s73","s74","s75","s76","s77","s78","s79","s80","s81","s82","s83","s84","s85","s86","s87","s88","s89","s90","s91","s92","s93","s94","s95","s96","s97","s98","s99","s100","s101","vcc","scc","memory");
}

extern "C" void kernel_launch(void* const* d_in, const int* in_sizes, int n_in, void* d_out, int out_size,
                              void* d_ws, size_t ws_size, hipStream_t stream) {
  (void)in_sizes; (void)n_in; (void)out_size; (void)ws_size;
  (void)hipFuncSetAttribute((const void*)fused_block_kernel, hipFuncAttributeMaxDynamicSharedMemorySize, 163840);
  fused_block_kernel<<<256, 512, 163840, stream>>>(d_in[0], d_in[1], d_in[2], d_in[4], d_in[5], d_in[6], d_out, d_ws);
}
```
